```python
import torch

def _softmax(x, axis=-1):
    return torch.softmax(x, dim=axis)
def _gelu(x):
    return torch.nn.functional.gelu(x, approximate="tanh")
def _silu(x):
    return torch.nn.functional.silu(x)
def _sigmoid(x):
    return torch.sigmoid(x)
def _layernorm(x, axis=-1, eps=1e-5):
    m = x.mean(dim=axis, keepdim=True)
    v = x.var(dim=axis, keepdim=True, unbiased=False)
    return (x - m) / torch.sqrt(v + eps)
def _scatter_add(tgt, idx, src, axis=0):
    return tgt.clone().index_add_(axis, idx, src)
def _segment_sum(src, seg, n):
    out = torch.zeros((n,) + src.shape[1:], dtype=src.dtype)
    return out.index_add_(0, seg, src)
def _pad(x, pw):
    flat = tuple(v for pair in reversed(pw) for v in pair)
    return torch.nn.functional.pad(x, flat)

kernel_name = 'parallel_sum__sliding_block__glu_gated_bilinear__prompt'

def setup_inputs():

    _w = (lambda a: (lambda t: t.to(torch.bfloat16) if t.is_floating_point() else t)(torch.as_tensor(a)))
    def _A_setup():
        B, S, D = 1, 2048, 640
        return {
            "x": _w((lambda *s: torch.randn(*s, dtype=torch.bfloat16) * 0.05)(B, S, D)),
            "wqkv": _w((lambda *s: torch.randn(*s, dtype=torch.bfloat16) * 0.05)(D, 3 * D)),
            "wo": _w((lambda *s: torch.randn(*s, dtype=torch.bfloat16) * 0.05)(D, D)),
        }
    _A_in = _A_setup()

    def _B_setup():
        B, S, D, F = 1, 2048, 640, 1728
        return {
            "x": _w((lambda *s: torch.randn(*s, dtype=torch.bfloat16) * 0.05)(B, S, D)),
            "w_gate": _w((lambda *s: torch.randn(*s, dtype=torch.bfloat16) * 0.8)(D, F) * (D ** -0.5)),
            "w_up": _w((lambda *s: torch.randn(*s, dtype=torch.bfloat16) * 0.8)(D, F) * (D ** -0.5)),
            "w_down": _w((lambda *s: torch.randn(*s, dtype=torch.bfloat16) * 0.8)(F, D) * (F ** -0.5)),
        }
    _B_in = _B_setup()

    return {
        "A_x": _A_in["x"],
        "A_wqkv": _A_in["wqkv"],
        "A_wo": _A_in["wo"],
        "B_x": _B_in["x"],
        "B_w_gate": _B_in["w_gate"],
        "B_w_up": _B_in["w_up"],
        "B_w_down": _B_in["w_down"],
    }

def reference(A_x, A_wqkv, A_wo, B_x, B_w_gate, B_w_up, B_w_down):
    def _A_ref(x, wqkv, wo):
        B, S, D = x.shape
        H = 32; Dh = D // H; W = 256
        qkv = (lambda x, *d: x.permute(*d))((x @ wqkv).reshape(B, S, 3, H, Dh), 2, 0, 3, 1, 4)
        q, k, v = qkv[0], qkv[1], qkv[2]
        scores = torch.einsum("bhqd,bhkd->bhqk", q, k) * (Dh ** -0.5)
        idx = torch.arange(S)
        mask = (idx[:, None] - idx[None, :] >= 0) & (idx[:, None] - idx[None, :] < W)
        scores = torch.where(mask[None, None], scores, -1e9)
        p = _softmax(scores, axis=-1)
        o = torch.einsum("bhqk,bhkd->bhqd", p, v)
        return (lambda x, *d: x.permute(*d))(o, 0, 2, 1, 3).reshape(B, S, D) @ wo

    def _B_ref(x, w_gate, w_up, w_down):
        g = torch.einsum("bsd,df->bsf", x, w_gate)
        u = torch.einsum("bsd,df->bsf", x, w_up)
        h = g * u
        return torch.einsum("bsf,fd->bsd", h, w_down)

    x = A_x
    xn = _layernorm(x, axis=-1)
    return x + _A_ref(xn, A_wqkv, A_wo) + _B_ref(xn, B_w_gate, B_w_up, B_w_down)
```

```cpp
#include <hip/hip_runtime.h>
#include <hip/hip_bf16.h>
typedef __hip_bfloat16 bf16;
constexpr int S = 2048, D = 640, F = 1728, H = 32, DH = 20, W = 256;
__device__ inline float b2f(bf16 v) { return __bfloat162float(v); }
__global__ void k_ln(const bf16* x, float* xn) {
  int row = blockIdx.x; int t = threadIdx.x;
  float s = 0, q = 0;
  for (int i = t; i < D; i += 64) { float v = b2f(x[row * D + i]); s += v; }
  for (int o = 32; o; o >>= 1) s += __shfl_xor(s, o);
  float m = s / D;
  for (int i = t; i < D; i += 64) { float v = b2f(x[row * D + i]) - m; q += v * v; }
  for (int o = 32; o; o >>= 1) q += __shfl_xor(q, o);
  float r = rsqrtf(q / D + 1e-5f);
  for (int i = t; i < D; i += 64) xn[row * D + i] = (b2f(x[row * D + i]) - m) * r;
}
__global__ void k_gemm(const float* A, const bf16* B, float* C, int M, int N, int K) {
  __shared__ float As[16][17], Bs[16][17];
  int tx = threadIdx.x & 15, ty = threadIdx.x >> 4;
  int row = blockIdx.y * 16 + ty, col = blockIdx.x * 16 + tx;
  float acc = 0;
  for (int k0 = 0; k0 < K; k0 += 16) {
    As[ty][tx] = A[row * K + k0 + tx];
    Bs[ty][tx] = b2f(B[(k0 + ty) * N + col]);
    __syncthreads();
    for (int k = 0; k < 16; ++k) acc += As[ty][k] * Bs[k][tx];
    __syncthreads();
  }
  C[row * N + col] = acc;
}
__global__ void k_attn(const float* qkv, float* o) {
  int q = blockIdx.x, h = blockIdx.y, t = threadIdx.x;
  __shared__ float p[256]; __shared__ float red[256];
  int key = q - t;
  float s = -1e30f;
  if (key >= 0) { s = 0; for (int d = 0; d < DH; ++d) s += qkv[q * 1920 + h * DH + d] * qkv[key * 1920 + 640 + h * DH + d]; s *= 0.22360679775f; }
  red[t] = s; __syncthreads();
  for (int o2 = 128; o2; o2 >>= 1) { if (t < o2) red[t] = fmaxf(red[t], red[t + o2]); __syncthreads(); }
  float m = red[0]; __syncthreads();
  float e = key >= 0 ? __expf(s - m) : 0.f; p[t] = e; red[t] = e; __syncthreads();
  for (int o2 = 128; o2; o2 >>= 1) { if (t < o2) red[t] += red[t + o2]; __syncthreads(); }
  float l = red[0];
  if (t < DH) { float acc = 0; for (int j = 0; j < 256; ++j) { int k2 = q - j; if (k2 >= 0) acc += p[j] * qkv[k2 * 1920 + 1280 + h * DH + t]; } o[q * D + h * DH + t] = acc / l; }
}
__global__ void k_mul(const float* g, const float* u, float* h, int n) { int i = blockIdx.x * 256 + threadIdx.x; if (i < n) h[i] = g[i] * u[i]; }
__global__ void k_fin(const bf16* x, const float* a, const float* b, bf16* out, int n) { int i = blockIdx.x * 256 + threadIdx.x; if (i < n) out[i] = __float2bfloat16(b2f(x[i]) + a[i] + b[i]); }
extern "C" void kernel_launch(void* const* d_in, const int* in_sizes, int n_in, void* d_out, int out_size, void* d_ws, size_t ws_size, hipStream_t stream) {
  const bf16* x = (const bf16*)d_in[0]; const bf16* wqkv = (const bf16*)d_in[1]; const bf16* wo = (const bf16*)d_in[2];
  const bf16* wg = (const bf16*)d_in[4]; const bf16* wu = (const bf16*)d_in[5]; const bf16* wd = (const bf16*)d_in[6];
  float* ws = (float*)d_ws;
  float* xn = ws; float* qkv = xn + S * D; float* o = qkv + S * 1920; float* g = o + S * D; float* u = g + S * F; float* h = u + S * F; float* a = h + S * F; float* b = a + S * D;
  k_ln<<<S, 64, 0, stream>>>(x, xn);
  k_gemm<<<dim3(1920 / 16, S / 16), 256, 0, stream>>>(xn, wqkv, qkv, S, 1920, D);
  k_attn<<<dim3(S, H), 256, 0, stream>>>(qkv, o);
  k_gemm<<<dim3(D / 16, S / 16), 256, 0, stream>>>(o, wo, a, S, D, D);
  k_gemm<<<dim3(F / 16, S / 16), 256, 0, stream>>>(xn, wg, g, S, F, D);
  k_gemm<<<dim3(F / 16, S / 16), 256, 0, stream>>>(xn, wu, u, S, F, D);
  k_mul<<<(S * F + 255) / 256, 256, 0, stream>>>(g, u, h, S * F);
  k_gemm<<<dim3(D / 16, S / 16), 256, 0, stream>>>(h, wd, b, S, D, F);
  k_fin<<<(S * D + 255) / 256, 256, 0, stream>>>(x, a, b, (bf16*)d_out, S * D);
}
```

```cpp
#include <hip/hip_runtime.h>
#include <hip/hip_bf16.h>

typedef short bf16x8 __attribute__((ext_vector_type(8)));
typedef short s16x4 __attribute__((ext_vector_type(4)));
typedef __bf16 bfv8 __attribute__((ext_vector_type(8)));
typedef __bf16 bfv2 __attribute__((ext_vector_type(2)));
typedef float f32x16 __attribute__((ext_vector_type(16)));
typedef float f32x4 __attribute__((ext_vector_type(4)));
typedef float f32x2 __attribute__((ext_vector_type(2)));
typedef unsigned u32x4 __attribute__((ext_vector_type(4)));
typedef unsigned u32x2 __attribute__((ext_vector_type(2)));
typedef unsigned long long u64;
#define LAS __attribute__((address_space(3)))
#define GAS __attribute__((address_space(1)))
#define DI __device__ __forceinline__
typedef LAS char* ldsp;
typedef __amdgpu_buffer_rsrc_t rsrc_t;
#define RLX_AGENT __ATOMIC_RELAXED, __HIP_MEMORY_SCOPE_AGENT
constexpr int AUX_SC1 = 16;

constexpr int S = 2048, D = 640, F = 1728, NH = 32, DH = 20;
constexpr int NP = 74;
constexpr size_t OFF_Q = 0, QK_BYTES = (size_t)16 * 64 * 2560;
constexpr size_t OFF_K = OFF_Q + QK_BYTES;
constexpr size_t OFF_V = OFF_K + QK_BYTES, V_BYTES = (size_t)16 * 64 * 3072;
constexpr size_t OFF_ACT = OFF_V + V_BYTES, ACT_BYTES = (size_t)32 * NP * 4096;
constexpr size_t OFF_WT = OFF_ACT + ACT_BYTES, WT_BYTES = (size_t)8 * NP * 5120;
constexpr size_t OFF_CSG = OFF_WT + WT_BYTES;
constexpr size_t OFF_RSG = OFF_CSG + (size_t)15 * 16 * 384 * 8;
constexpr size_t OFF_CNT = OFF_RSG + 4096 * 8;
constexpr size_t WS_USED = OFF_CNT + 16384;
constexpr size_t OFF_STAMP = WS_USED;
#define STAMPW(k, p, wv) do {} while (0)
#define STAMP(k, p) STAMPW(k, p, 0)

DI f32x16 mfma32(bf16x8 a, bf16x8 b, f32x16 c) { return __builtin_amdgcn_mfma_f32_32x32x16_bf16(__builtin_bit_cast(bfv8, a), __builtin_bit_cast(bfv8, b), c, 0, 0, 0); }
DI f32x16 mfma32k8(s16x4 a, s16x4 b, f32x16 c) { return __builtin_amdgcn_mfma_f32_32x32x8bf16_1k(a, b, c, 0, 0, 0); }
DI f32x4 mfma16(bf16x8 a, bf16x8 b, f32x4 c) { return __builtin_amdgcn_mfma_f32_16x16x32_bf16(__builtin_bit_cast(bfv8, a), __builtin_bit_cast(bfv8, b), c, 0, 0, 0); }
DI unsigned pk2(float lo, float hi) { bfv2 v = {(__bf16)lo, (__bf16)hi}; return __builtin_bit_cast(unsigned, v); }
DI float lo16(unsigned v) { return __uint_as_float(v << 16); }
DI float hi16(unsigned v) { return __uint_as_float(v & 0xffff0000u); }
DI float dot2(unsigned a, unsigned b, float c) { return __builtin_amdgcn_fdot2_f32_bf16(__builtin_bit_cast(bfv2, a), __builtin_bit_cast(bfv2, b), c, false); }
DI s16x4 trd(ldsp p) { return __builtin_amdgcn_ds_read_tr16_b64_v4i16((LAS s16x4*)p); }
DI bf16x8 cat8(s16x4 a, s16x4 b) { return __builtin_shufflevector(a, b, 0, 1, 2, 3, 4, 5, 6, 7); }
DI void dma16(unsigned ldsaddr, unsigned voff, rsrc_t rs, unsigned soff) {
  asm volatile("s_mov_b32 m0, %0\n\ts_nop 0\n\tbuffer_load_dwordx4 %1, %2, %3 offen lds" ::"s"(ldsaddr), "v"(voff), "s"(rs), "s"(soff) : "memory");
}
#define WAITBAR(N) asm volatile("s_waitcnt vmcnt(" #N ") lgkmcnt(0)\n\ts_barrier" ::: "memory")
#define SB() __builtin_amdgcn_sched_barrier(0)
DI rsrc_t mkrs(const void* p, unsigned bytes) { return __builtin_amdgcn_make_buffer_rsrc((void*)p, 0, bytes, 0x00020000); }
DI u64 ld_gran(const char* p) { return __hip_atomic_load((const GAS u64*)p, RLX_AGENT); }
DI void st_gran(char* p, unsigned tag, float v) { __hip_atomic_store((GAS u64*)p, ((u64)tag << 32) | __float_as_uint(v), RLX_AGENT); }
DI unsigned ld_epoch(const char* p) { unsigned v; asm volatile("s_load_dword %0, %1, 0x0\n\ts_waitcnt lgkmcnt(0)" : "=s"(v) : "s"(p) : "memory"); return v; }
DI void ld_epoch_issue(unsigned& v, const char* p) { asm volatile("s_load_dword %0, %1, 0x0" : "=s"(v) : "s"(p) : "memory"); }
DI void ld_epoch_wait(unsigned& v) { asm volatile("s_waitcnt lgkmcnt(0)" : "+s"(v) :: "memory"); }
DI int zchunk(int ch) { const int j = (ch * 52) >> 8, c = ch - 5 * j; return 5 * j + (c == 2 ? 3 : (c == 3 ? 4 : (c == 4 ? 2 : c))); }

constexpr int K1_XS = 0, K1_WS = 49152, K1_RS = 147456, K1_CS = 148480, K1_LDS = 150016;
template <int NT> struct K1Frag { bf16x8 x[2]; bf16x8 w[NT]; };
struct K1Ctx {
  unsigned wa[3], xa0, xa1, xb0, xb1;
  unsigned vx, vw0, vw1, vw2, ldw32, dA, dB, dC, dX;
  unsigned vcs, vrs;
  rsrc_t rx, rA, rB, rC, rws;
};

template <int NT, int MODE>
DI void k1_body(int i, f32x16 (&acc)[2][3], K1Frag<NT>& FA, const K1Ctx& c, unsigned xslot, unsigned xslot_next, unsigned wpair, u64 (&g0)[16], u64& g1) {
  ldsp L = (ldsp)0;
  auto rdW1 = [&](unsigned off, int n) { return cat8(trd(L + c.wa[n] + off), trd(L + c.wa[n] + off + 1536)); };
  auto rdX1 = [&](unsigned a) { return *(const LAS bf16x8*)(L + a); };
  auto gld = [&](int k) {
    if (k < 16) g0[k] = __builtin_bit_cast(u64, __builtin_amdgcn_raw_buffer_load_b64(c.rws, c.vcs + k * 3072, 0, AUX_SC1));
    else g1 = __builtin_bit_cast(u64, __builtin_amdgcn_raw_buffer_load_b64(c.rws, c.vrs, 0, AUX_SC1));
  };
  auto phase = [&](const K1Frag<NT>& cur, K1Frag<NT>& nxt, unsigned xo, unsigned wo, bool rd, auto aux) {
    acc[0][0] = mfma32(cur.w[0], cur.x[0], acc[0][0]);
    if (rd) { nxt.x[0] = rdX1(xo); nxt.x[1] = rdX1(xo + 4096); }
    SB(); aux(0); SB();
    acc[0][1] = mfma32(cur.w[1], cur.x[0], acc[0][1]);
    if (rd) nxt.w[0] = rdW1(wo, 0);
    SB(); aux(1); SB();
    if constexpr (NT == 3) acc[0][2] = mfma32(cur.w[2], cur.x[0], acc[0][2]); else acc[1][0] = mfma32(cur.w[0], cur.x[1], acc[1][0]);
    if (rd) nxt.w[1] = rdW1(wo, 1);
    SB(); aux(2); SB();
    if constexpr (NT == 3) acc[1][0] = mfma32(cur.w[0], cur.x[1], acc[1][0]); else acc[1][1] = mfma32(cur.w[1], cur.x[1], acc[1][1]);
    if constexpr (NT == 3) { if (rd) nxt.w[2] = rdW1(wo, 2); }
    SB(); aux(3); SB();
    if constexpr (NT == 3) acc[1][1] = mfma32(cur.w[1], cur.x[1], acc[1][1]);
    SB(); aux(4); SB();
    if constexpr (NT == 3) acc[1][2] = mfma32(cur.w[2], cur.x[1], acc[1][2]);
    SB();
  };
  auto none = [&](int) {};
  K1Frag<NT> FB;
  const unsigned w0 = wpair, w1 = wpair + 24576;
  SB();
  phase(FA, FB, c.xb0 + xslot, w0 + 6144, true, none);
  if constexpr (MODE == 0) WAITBAR(10); else if constexpr (MODE == 1) WAITBAR(8); else WAITBAR(17);
  SB();
  {
    const unsigned so = (unsigned)(2 * i + 4) * c.ldw32;
    phase(FB, FA, c.xa1 + xslot, w1, true, [&](int k) {
      if constexpr (MODE == 0) { if (k == 0) dma16(w0 + c.dA, c.vw0, c.rA, so); if (k == 1) dma16(w0 + c.dB, c.vw1, c.rB, so); if (k == 2) dma16(w0 + c.dC, c.vw2, c.rC, so); }
      if constexpr (MODE == 1) gld(k);
    });
  }
  phase(FA, FB, c.xb1 + xslot, w1 + 6144, true, [&](int k) { if constexpr (MODE == 1) gld(5 + k); });
  if constexpr (MODE != 2) {
    if constexpr (MODE == 0) WAITBAR(8); else WAITBAR(13);
    SB();
    const unsigned so = (unsigned)(2 * i + 5) * c.ldw32;
    const unsigned sx = (unsigned)(i < 7 ? i + 3 : 9) * 128;
    phase(FB, FA, c.xa0 + xslot_next, wpair ^ 49152, true, [&](int k) {
      if constexpr (MODE == 0) {
        if (k == 0) dma16(w1 + c.dA, c.vw0, c.rA, so); if (k == 1) dma16(w1 + c.dB, c.vw1, c.rB, so); if (k == 2) dma16(w1 + c.dC, c.vw2, c.rC, so);
        if (k == 3) dma16(xslot + c.dX, c.vx, c.rx, sx); if (k == 4) dma16(xslot + c.dX + 8192, c.vx, c.rx, sx + 64 * 1280);
      }
      if constexpr (MODE == 1) { gld(10 + k); if (k == 4) { gld(15); gld(16); } }
    });
  } else {
    phase(FB, FA, 0, 0, false, none);
  }
}

template <int NT>
DI void k1_loop(f32x16 (&acc)[2][3], const K1Ctx& c, u64 (&g0)[16], u64& g1, char* ws) {
#define BST(i) do {} while (0)
  ldsp L = (ldsp)0;
  K1Frag<NT> FA;
  FA.x[0] = *(const LAS bf16x8*)(L + c.xa0); FA.x[1] = *(const LAS bf16x8*)(L + c.xa0 + 4096);
#pragma unroll
  for (int n = 0; n < NT; ++n) FA.w[n] = cat8(trd(L + c.wa[n]), trd(L + c.wa[n] + 1536));
  unsigned xs = 0, xsn = 16384, wp = 0;
  for (int i = 0; i < 8; ++i) {
    BST(i);
    k1_body<NT, 0>(i, acc, FA, c, xs, xsn, wp, g0, g1);
    xs = xsn; xsn = xsn == 32768 ? 0 : xsn + 16384; wp ^= 49152;
  }
  BST(8);
  k1_body<NT, 1>(8, acc, FA, c, xs, xsn, wp, g0, g1);
  xs = xsn; wp ^= 49152;
  BST(9);
  k1_body<NT, 2>(9, acc, FA, c, xs, xsn, wp, g0, g1);
  BST(10);
}

DI int krow(int P, int slot) {
  const int g4 = slot >> 3, j = slot & 7;
  if (P < 54) return 32 * P + 16 * (g4 >> 1) + 4 * (g4 & 1) + (j & 3) + 8 * (j >> 2);
  const int Pp = P - 54;
  int head, i;
  if (Pp < 16) { head = 2 * Pp + (g4 >> 1); i = (j & 3) + 8 * (j >> 2) + 4 * (g4 & 1); } else { head = 2 * (4 * (Pp - 16) + g4) + (j >> 2); i = 16 + (j & 3); }
  const int d = (head & 1) ? (i < 16 ? i + 4 : i - 16) : i;
  return head * 20 + d;
}

DI float dppadd(float v, int ctrl_sel) {
  int x = __builtin_bit_cast(int, v), y;
  if (ctrl_sel == 0) y = __builtin_amdgcn_update_dpp(0, x, 0xB1, 0xf, 0xf, true);
  else if (ctrl_sel == 1) y = __builtin_amdgcn_update_dpp(0, x, 0x4E, 0xf, 0xf, true);
  else y = __builtin_amdgcn_update_dpp(0, x, 0x141, 0xf, 0xf, true);
  return v + __builtin_bit_cast(float, y);
}
DI void k1_helper(int r  , unsigned tag, const unsigned short* __restrict__ wd, const unsigned short* __restrict__ wo, char* __restrict__ ws) {
  const int tid = threadIdx.x, l = tid & 63, w = __builtin_amdgcn_readfirstlane(tid >> 6);
  if (r == 0 && tid < 8 * 64) ((unsigned*)(ws + OFF_CNT + 8192))[tid] = 0u;
  ldsp L = (ldsp)0 + w * 3 * 5120;
  const int g4 = l >> 4, q4 = (l & 15) >> 2, p = l & 3;
#pragma unroll 1
  for (int rnd = 0; rnd < 2; ++rnd) {
    u32x4 v[3][5];
#pragma unroll
    for (int uu = 0; uu < 3; ++uu) {
      int unit = (r * 8 + w) + 128 * (3 * rnd + uu); if (unit >= NP * 8) unit = 0;
      const int P = unit >> 3, nb = unit & 7;
      const unsigned short* src = P < 54 ? wd : wo;
#pragma unroll
      for (int i = 0; i < 5; ++i) { const int q = 64 * i + l, slot = q / 10, ch = q - 10 * slot; v[uu][i] = *(const u32x4*)(src + (size_t)krow(P, slot) * D + nb * 80 + ch * 8); }
    }
#pragma unroll
    for (int uu = 0; uu < 3; ++uu)
#pragma unroll
      for (int i = 0; i < 5; ++i) *(LAS u32x4*)(L + uu * 5120 + (64 * i + l) * 16) = v[uu][i];
    asm volatile("s_waitcnt lgkmcnt(0)" ::: "memory");
#pragma unroll
    for (int uu = 0; uu < 3; ++uu) {
      const int unit = (r * 8 + w) + 128 * (3 * rnd + uu);
      if (unit < NP * 8) {
        const int P = unit >> 3, nb = unit & 7;
#pragma unroll
        for (int nt = 0; nt < 5; ++nt) {
          const int colg = nt < 4 ? 32 * (nt >> 1) + 8 * p + 4 * (nt & 1) : 64 + 4 * p;
          ldsp a = L + uu * 5120 + (g4 * 8 + q4) * 160 + colg * 2;
          const bf16x8 f = cat8(trd(a), trd(a + 4 * 160));
          *(bf16x8*)(ws + OFF_WT + ((size_t)(nb * NP + P) * 5 + nt) * 1024 + l * 16) = f;
        }
      }
    }
    asm volatile("s_waitcnt lgkmcnt(0)" ::: "memory");
  }
  if (r == 0 && w == 0) {
    const GAS unsigned* st = (const GAS unsigned*)(ws + OFF_CNT + 4096);
    for (unsigned spins = 0; spins < (1u << 20); ++spins) {
      bool ok = true;
#pragma unroll
      for (int i = 0; i < 4; ++i) ok &= __hip_atomic_load(st + i * 64 + l, RLX_AGENT) == tag;
      if (__all(ok)) break;
      __builtin_amdgcn_s_sleep(2);
    }
    if (l == 0) __hip_atomic_store((GAS unsigned*)(ws + OFF_CNT), tag, RLX_AGENT);
  }
}

__global__ __launch_bounds__(512, 2) void k1_gemm1(const unsigned short* __restrict__ x, const unsigned short* __restrict__ wqkv,
                                                   const unsigned short* __restrict__ wg, const unsigned short* __restrict__ wu,
                                                   const unsigned short* __restrict__ wd, const unsigned short* __restrict__ wo, char* __restrict__ ws) {
  extern __shared__ __attribute__((aligned(16))) char lds[];
  const int b = blockIdx.x, xcd = b & 7, jj = b >> 3;
  const int tid = threadIdx.x, l = tid & 63, w = __builtin_amdgcn_readfirstlane(tid >> 6);
  asm volatile("" ::"s"(x), "s"(wqkv), "s"(wg), "s"(wu), "s"(wd), "s"(wo), "s"(ws));
  unsigned epoch; ld_epoch_issue(epoch, ws + OFF_CNT);
  STAMP(0, 0);
  if (jj >= 30) {
    ld_epoch_wait(epoch);
    const unsigned tag = epoch + 1u;
    if (tid == 0) __hip_atomic_store((GAS unsigned*)(ws + OFF_CNT + 4096) + b, tag, RLX_AGENT);
    k1_helper(xcd * 2 + (jj - 30), tag, wd, wo, ws);
    STAMP(0, 5);
    return;
  }
  const int idx = (xcd >> 1) * 30 + jj, cb = idx >> 3, panel = (xcd & 1) * 8 + (idx & 7);
  const bool isffn = cb >= 6;
  K1Ctx c;
  c.rx = mkrs((const char*)x + (size_t)panel * 128 * 1280, 128 * 1280);
  const int xrow = 8 * w + (l >> 3);
  c.vx = xrow * 1280 + (((l & 7) ^ ((xrow >> 1) & 7)) << 4);
  c.dX = K1_XS + w * 1024;
  dma16(c.dX, c.vx, c.rx, 0); dma16(c.dX + 8192, c.vx, c.rx, 64 * 1280);
  SB();
  const int wm = w & 1, wn = w >> 1;
  const unsigned ldw = isffn ? 3456u : 3840u;
  const int qbase = (cb >> 1) * 640 + (cb & 1) * 320;
  const char* pA = isffn ? (const char*)wg + (cb - 6) * 384 : (const char*)wqkv + qbase * 2;
  const char* pB = isffn ? (const char*)wu + (cb - 6) * 384 : pA;
  const unsigned wbytes = 640u * ldw - (isffn ? (cb - 6) * 384 : qbase * 2);
  c.rA = mkrs(pA, wbytes); c.rB = mkrs(pB, wbytes); c.rC = w < 4 ? c.rA : c.rB;
  auto wv = [&](int i, int mat) {
    const int q = 64 * i + l, k = q / 24, p = q - 24 * k, s = p ^ (2 * (((k >> 1) & 1) | (mat << 1)));
    int ch16 = s;
    if (!isffn) { const int t = s >> 1; ch16 = t < 10 ? zchunk(4 * t + 2 * mat + (s & 1)) : 0; }
    return (unsigned)(k * ldw + (ch16 << 4));
  };
  const int iC = w < 4 ? 8 + w : 4 + w, mC = w < 4 ? 0 : 1;
  c.vw0 = wv(w, 0); c.vw1 = wv(w, 1); c.vw2 = wv(iC, mC);
  c.dA = K1_WS + w * 1024; c.dB = K1_WS + 12288 + w * 1024; c.dC = K1_WS + mC * 12288 + iC * 1024;
  c.ldw32 = 32 * ldw;
  u32x4 sv[10];
  const int schunk = 8 * w + (l >> 3);
  const int sr9 = 8 * (w - 6) + (l >> 3), srow = 9 * cb + sr9;
  const bool do_cs = w < 6 && (isffn || schunk < 40), do_rs = w >= 6 && sr9 < 9 && srow < 128;
  {
    const unsigned vo = (unsigned)(40 * panel + 5 * (l & 7)) * ldw + (unsigned)((schunk >= 24 && isffn) ? schunk - 24 : schunk) * 16u;
    const rsrc_t rs = w >= 6 ? c.rx : ((w >= 3 && isffn) ? c.rB : c.rA);
    const unsigned va = w >= 6 ? (do_rs ? (unsigned)srow * 1280u + (l & 7) * 16u : 0x7ffffff0u) : (do_cs ? vo : 0x7ffffff0u);
    const unsigned st = w >= 6 ? 128u : ldw;
    const unsigned vb = w >= 6 ? va + 640u : 0x7ffffff0u;
    asm volatile("buffer_load_dwordx4 %0, %10, %12, 0 offen\n\tbuffer_load_dwordx4 %1, %10, %12, %13 offen\n\tbuffer_load_dwordx4 %2, %10, %12, %14 offen\n\t"
                 "buffer_load_dwordx4 %3, %10, %12, %15 offen\n\tbuffer_load_dwordx4 %4, %10, %12, %16 offen\n\t"
                 "buffer_load_dwordx4 %5, %11, %12, 0 offen\n\tbuffer_load_dwordx4 %6, %11, %12, %13 offen\n\tbuffer_load_dwordx4 %7, %11, %12, %14 offen\n\t"
                 "buffer_load_dwordx4 %8, %11, %12, %15 offen\n\tbuffer_load_dwordx4 %9, %11, %12, %16 offen"
                 : "=&v"(sv[0]), "=&v"(sv[1]), "=&v"(sv[2]), "=&v"(sv[3]), "=&v"(sv[4]), "=&v"(sv[5]), "=&v"(sv[6]), "=&v"(sv[7]), "=&v"(sv[8]), "=&v"(sv[9])
                 : "v"(va), "v"(vb), "s"(rs), "s"(st), "s"(2 * st), "s"(3 * st), "s"(4 * st) : "memory");
  }
  dma16(c.dA, c.vw0, c.rA, 0); dma16(c.dB, c.vw1, c.rB, 0); dma16(c.dC, c.vw2, c.rC, 0);
  dma16(c.dA + 24576, c.vw0, c.rA, c.ldw32); dma16(c.dB + 24576, c.vw1, c.rB, c.ldw32); dma16(c.dC + 24576, c.vw2, c.rC, c.ldw32);
  dma16(c.dX + 16384, c.vx, c.rx, 128); dma16(c.dX + 16384 + 8192, c.vx, c.rx, 128 + 64 * 1280);
  dma16(c.dA + 49152, c.vw0, c.rA, 2 * c.ldw32); dma16(c.dB + 49152, c.vw1, c.rB, 2 * c.ldw32); dma16(c.dC + 49152, c.vw2, c.rC, 2 * c.ldw32);
  dma16(c.dA + 73728, c.vw0, c.rA, 3 * c.ldw32); dma16(c.dB + 73728, c.vw1, c.rB, 3 * c.ldw32); dma16(c.dC + 73728, c.vw2, c.rC, 3 * c.ldw32);
  dma16(c.dX + 32768, c.vx, c.rx, 256); dma16(c.dX + 32768 + 8192, c.vx, c.rx, 256 + 64 * 1280);
  {
    asm volatile("s_waitcnt vmcnt(16)" : "+v"(sv[0]), "+v"(sv[1]), "+v"(sv[2]), "+v"(sv[3]), "+v"(sv[4]), "+v"(sv[5]), "+v"(sv[6]), "+v"(sv[7]), "+v"(sv[8]), "+v"(sv[9]) :: "memory");
    ld_epoch_wait(epoch);
    const unsigned tag = epoch + 1u;
    if (tid == 0) __hip_atomic_store((GAS unsigned*)(ws + OFF_CNT + 4096) + b, tag, RLX_AGENT);
    if (w < 6) {
      float s8[8];
#pragma unroll
      for (int e = 0; e < 4; ++e) {
        s8[2 * e] = ((lo16(sv[0][e]) + lo16(sv[1][e])) + (lo16(sv[2][e]) + lo16(sv[3][e]))) + lo16(sv[4][e]);
        s8[2 * e + 1] = ((hi16(sv[0][e]) + hi16(sv[1][e])) + (hi16(sv[2][e]) + hi16(sv[3][e]))) + hi16(sv[4][e]);
      }
#pragma unroll
      for (int st = 0; st < 3; ++st)
#pragma unroll
        for (int e = 0; e < 8; ++e) s8[e] = dppadd(s8[e], st);
      float mine = s8[0];
#pragma unroll
      for (int e = 1; e < 8; ++e) mine = (l & 7) == e ? s8[e] : mine;
      if (do_cs) st_gran(ws + OFF_CSG + ((size_t)(cb * 16 + panel) * 384 + schunk * 8 + (l & 7)) * 8, tag, mine);
    } else {
      float a0 = 0, a1 = 0, q0 = 0, q1 = 0;
#pragma unroll
      for (int j = 0; j < 10; ++j) {
        a0 = dot2(sv[j][0], 0x3f803f80u, a0); a1 = dot2(sv[j][1], 0x3f803f80u, a1); a0 = dot2(sv[j][2], 0x3f803f80u, a0); a1 = dot2(sv[j][3], 0x3f803f80u, a1);
      }
      const float mean = dppadd(dppadd(dppadd(a0 + a1, 0), 1), 2) * (1.f / 640.f);
#pragma unroll
      for (int j = 0; j < 10; ++j)
#pragma unroll
        for (int e = 0; e < 4; ++e) { const float d0 = lo16(sv[j][e]) - mean, d1 = hi16(sv[j][e]) - mean; q0 = fmaf(d0, d0, q0); q1 = fmaf(d1, d1, q1); }
      const float rstd = __builtin_amdgcn_rsqf(dppadd(dppadd(dppadd(q0 + q1, 0), 1), 2) * (1.f / 640.f) + 1e-5f);
      if (do_rs && (l & 7) < 2) st_gran(ws + OFF_RSG + (size_t)((panel * 128 + srow) * 2 + (l & 1)) * 8, tag, (l & 1) ? rstd : mean);
    }
  }
  const unsigned tag = epoch + 1u;
  const int t0 = isffn ? 3 * wn : (wn < 2 ? 3 * wn : 2 * wn + 2);
  const bool three = isffn || wn < 2;
  const int h = l >> 5, blk = (l >> 4) & 1, q4 = (l & 15) >> 2, p = l & 3;
  const unsigned swz = 2 * (((q4 >> 1) & 1) | (blk << 1));
  const int pc = isffn ? (p >> 1) : (p & 1), ph = isffn ? (p & 1) : (p >> 1);
#pragma unroll
  for (int nt = 0; nt < 3; ++nt) c.wa[nt] = (unsigned)(K1_WS + blk * 12288 + (8 * h + q4) * 384 + (((2 * (t0 + nt) + pc) ^ swz) << 4) + ph * 8);
  const int xr = 64 * wm + (l & 31);
  auto xadr = [&](int ks4) { return (unsigned)(K1_XS + xr * 128 + (((2 * ks4 + h) ^ ((xr >> 1) & 7)) << 4)); };
  c.xa0 = xadr(0); c.xb0 = xadr(1); c.xa1 = xadr(2); c.xb1 = xadr(3);
  const int cu = tid >> 5, ci = tid & 31;
  int ccol;
  if (isffn) ccol = (ci >> 4) * 192 + 16 * cu + (ci & 15);
  else { const int pp = (ci >> 2) & 3, ch = 4 * cu + 2 * (ci >> 4) + (pp & 1); ccol = cu < 10 ? 8 * zchunk(ch) + 4 * (pp >> 1) + (ci & 3) : 0; }
  c.rws = mkrs(ws, (unsigned)WS_USED);
  c.vcs = (unsigned)OFF_CSG + (unsigned)(cb * 16 * 384 + (tid < 384 ? ccol : 0)) * 8u;
  c.vrs = (unsigned)OFF_RSG + (unsigned)(panel * 256 + (tid & 255)) * 8u;
  f32x16 acc[2][3];
#pragma unroll
  for (int mt = 0; mt < 2; ++mt)
#pragma unroll
    for (int nt = 0; nt < 3; ++nt) acc[mt][nt] = (f32x16){};
  WAITBAR(13);
  STAMP(0, 1);
  u64 g0[16], g1;
  if (three) k1_loop<3>(acc, c, g0, g1, ws); else k1_loop<2>(acc, c, g0, g1, ws);
  STAMP(0, 2);
  {
    ldsp L = (ldsp)0;
    for (unsigned spins = 0; spins < (1u << 20); ++spins) {
      bool ok = (unsigned)(g1 >> 32) == tag;
#pragma unroll
      for (int i = 0; i < 16; ++i) ok &= (unsigned)(g0[i] >> 32) == tag;
      if (!isffn && tid >= 320) ok = (unsigned)(g1 >> 32) == tag;
      if (__all(ok)) break;
      __builtin_amdgcn_s_sleep(2);
#pragma unroll
      for (int i = 0; i < 16; ++i) g0[i] = __builtin_bit_cast(u64, __builtin_amdgcn_raw_buffer_load_b64(c.rws, c.vcs + i * 3072, 0, AUX_SC1));
      g1 = __builtin_bit_cast(u64, __builtin_amdgcn_raw_buffer_load_b64(c.rws, c.vrs, 0, AUX_SC1));
    }
    {
      float a = 0;
#pragma unroll
      for (int i = 0; i < 16; ++i) a += __uint_as_float((unsigned)g0[i]);
      if (tid < 384) *(LAS float*)(L + K1_CS + tid * 4) = a;
    }
    if (tid < 256) *(LAS unsigned*)(L + K1_RS + tid * 4) = (unsigned)g1;
    __syncthreads();
    const int m32 = l & 31;
    const rsrc_t rws = c.rws;
    f32x2 st[2];
#pragma unroll
    for (int mt = 0; mt < 2; ++mt) st[mt] = *(const LAS f32x2*)(L + K1_RS + (64 * wm + 32 * mt + m32) * 8);
#pragma unroll
    for (int nt = 0; nt < 3; ++nt) {
      if (nt < 2 || three) {
        const int u = t0 + nt;
        float cc[16];
#pragma unroll
        for (int g = 0; g < 4; ++g) { f32x4 t = *(const LAS f32x4*)(L + K1_CS + (u * 32 + 8 * g + 4 * h) * 4); cc[4 * g] = t[0]; cc[4 * g + 1] = t[1]; cc[4 * g + 2] = t[2]; cc[4 * g + 3] = t[3]; }
        if (isffn) {
          const int U = (cb - 6) * 12 + u, P = U >> 1, g4 = (U & 1) * 2 + h;
          const unsigned off = (unsigned)OFF_ACT + (((panel * 2 + wm) * NP + P) * 4 + (m32 >> 4)) * 1024 + g4 * 256 + (m32 & 15) * 16;
#pragma unroll
          for (int mt = 0; mt < 2; ++mt) {
            const float nm = -st[mt][0], r2 = st[mt][1] * st[mt][1];
            float hv[8];
#pragma unroll
            for (int j = 0; j < 8; ++j) hv[j] = (fmaf(nm, cc[j], acc[mt][nt][j]) * r2) * fmaf(nm, cc[j + 8], acc[mt][nt][j + 8]);
            u32x4 o = {pk2(hv[0], hv[1]), pk2(hv[2], hv[3]), pk2(hv[4], hv[5]), pk2(hv[6], hv[7])};
            __builtin_amdgcn_raw_buffer_store_b128(o, rws, off + mt * 2048, 0, AUX_SC1);
          }
        } else {
          const int mat = cb >> 1;
          const unsigned TS = mat == 2 ? 3072u : 2560u;
          const unsigned mbase = (unsigned)(mat == 2 ? OFF_V : (mat == 1 ? OFF_K : OFF_Q)) + (unsigned)((cb & 1) * 8 * 64 + panel * 4 + 2 * wm) * TS + m32 * 16;
          const float qs = mat == 0 ? 0.22360679774997896f * 1.4426950408889634f : 1.f;
#pragma unroll
          for (int hf = 0; hf < 2; ++hf) {
            const int ch = 4 * u + 2 * hf + h, j = (ch * 52) >> 8, cix = ch - 5 * j;
            const unsigned off = mbase + (unsigned)j * 64u * TS + cix * 512;
#pragma unroll
            for (int mt = 0; mt < 2; ++mt) {
              const float nm = -st[mt][0], sc = st[mt][1] * qs;
              float v[8];
#pragma unroll
              for (int e = 0; e < 8; ++e) v[e] = fmaf(nm, cc[8 * hf + e], acc[mt][nt][8 * hf + e]) * sc;
              u32x4 o = {pk2(v[0], v[1]), pk2(v[2], v[3]), pk2(v[4], v[5]), pk2(v[6], v[7])};
              __builtin_amdgcn_raw_buffer_store_b128(o, rws, off + mt * TS, 0, AUX_SC1);
              if (mat == 2 && cix == 4) __builtin_amdgcn_raw_buffer_store_b128((u32x4){0x00003f80u, 0u, 0u, 0u}, rws, off + mt * TS + 512, 0, AUX_SC1);
            }
          }
        }
      }
    }
  }
  STAMP(0, 3);
  STAMP(0, 4);
}

constexpr int A_KM = 0, A_KT = 16384, A_VM = 24576, A_VT = 40960, A_QM = 57344, A_QT = 65536, A_END = 69632;
DI void attn_store(rsrc_t rws, int head, int qt, const f32x16& o) {
  const int l = threadIdx.x & 63, h = l >> 5, q = l & 31;
  auto rr = __builtin_amdgcn_permlane32_swap(__float_as_uint(o[8]), __float_as_uint(o[8]), false, false);
  const float inv = __builtin_amdgcn_rcpf(__uint_as_float(rr[1]));
  u32x4 ov = {pk2(o[0] * inv, o[1] * inv), pk2(o[2] * inv, o[3] * inv), pk2(o[4] * inv, o[5] * inv), pk2(o[6] * inv, o[7] * inv)};
  const int tok = qt * 32 + q, panel = tok >> 6, mt = (tok >> 4) & 3, m16 = tok & 15;
  const unsigned base = (unsigned)OFF_ACT + ((panel * NP + 54) * 4 + mt) * 1024 + m16 * 16;
  __builtin_amdgcn_raw_buffer_store_b128(ov, rws, base + (head >> 1) * 4096 + ((head & 1) * 2 + h) * 256, 0, AUX_SC1);
  if (!h) __builtin_amdgcn_raw_buffer_store_b64((u32x2){pk2(o[8] * inv, o[9] * inv), pk2(o[10] * inv, o[11] * inv)}, rws,
                                                base + (16 + (head >> 3)) * 4096 + ((head >> 1) & 3) * 256 + (head & 1) * 8, 0, AUX_SC1);
}
DI void attn_task_short(rsrc_t rws, int head, int qt, int ql) {
  const int l = threadIdx.x & 63, h = l >> 5, q = l & 31, e = head & 1;
  const int jb = ql;
  ldsp L = (ldsp)0;
  const bf16x8 qa = *(const LAS bf16x8*)(L + A_QM + ql * 1024 + h * 512 + q * 16);
  s16x4 qb = *(const LAS s16x4*)(L + A_QT + ql * 512 + q * 16 + e * 8);
  if (h) qb = (s16x4){0, 0, 0, 0};
  const unsigned ka = A_KM + jb * 1024 + h * 512 + q * 16, kb = A_KT + jb * 512 + q * 16 + e * 8;
  const int j0 = 8 - qt;
  auto score = [&](int j, f32x16 c) {
    const bf16x8 a = *(const LAS bf16x8*)(L + ka + j * 1024);
    const s16x4 at = *(const LAS s16x4*)(L + kb + j * 512);
    c = mfma32(a, qa, c);
    return mfma32k8(at, qb, c);
  };
  const float NEG = -3.0e38f;
  f32x16 z = {};
  float m = NEG;
  {
    f32x16 s8 = score(8, z);
#pragma unroll
    for (int r = 0; r < 16; ++r) { const int key = (r & 3) + 8 * (r >> 2) + 4 * h; m = fmaxf(m, key > q ? NEG : s8[r]); }
    for (int j = j0; j < 8; ++j) {
      f32x16 s = score(j, z);
#pragma unroll
      for (int r = 0; r < 16; r += 2) m = fmaxf(fmaxf(m, s[r]), s[r + 1]);
    }
    auto rr = __builtin_amdgcn_permlane32_swap(__float_as_uint(m), __float_as_uint(m), false, false);
    m = fmaxf(__uint_as_float(rr[0]), __uint_as_float(rr[1]));
  }
  f32x16 cm;
#pragma unroll
  for (int r = 0; r < 16; ++r) cm[r] = -m;
  const int blk = (l >> 4) & 1, q4 = (l & 15) >> 2, p = l & 3;
  const unsigned va = (blk ? A_VT + (p == 0 ? e * 8 : 512 + (p >> 1) * 8) : A_VM + (p >> 1) * 512 + (p & 1) * 8) + jb * 1024 + (4 * h + q4) * 16;
  f32x16 o = {};
  for (int j = j0; j <= 8; ++j) {
    f32x16 s = score(j, cm);
    u32x4 p0, p1;
#pragma unroll
    for (int i = 0; i < 8; ++i) {
      const int key = ((2 * i) & 3) + 8 * ((2 * i) >> 2) + 4 * h;
      const float e0 = (j == 8 && key > q) ? 0.f : __builtin_amdgcn_exp2f(s[2 * i]), e1 = (j == 8 && key + 1 > q) ? 0.f : __builtin_amdgcn_exp2f(s[2 * i + 1]);
      if (i < 4) p0[i & 3] = pk2(e0, e1); else p1[i & 3] = pk2(e0, e1);
    }
    const unsigned off = va + (unsigned)j * 1024;
    o = mfma32(cat8(trd(L + off), trd(L + off + 128)), __builtin_bit_cast(bf16x8, p0), o);
    o = mfma32(cat8(trd(L + off + 256), trd(L + off + 384)), __builtin_bit_cast(bf16x8, p1), o);
  }
  attn_store(rws, head, qt, o);
}
DI void attn_task_full(rsrc_t rws, int head, int qt, int ql) {
  const int l = threadIdx.x & 63, h = l >> 5, q = l & 31, e = head & 1;
  const int jb = ql;
  ldsp L = (ldsp)0;
  const bf16x8 qa = *(const LAS bf16x8*)(L + A_QM + ql * 1024 + h * 512 + q * 16);
  s16x4 qb = *(const LAS s16x4*)(L + A_QT + ql * 512 + q * 16 + e * 8);
  const unsigned ka = A_KM + jb * 1024 + h * 512 + q * 16, kb = A_KT + jb * 512 + q * 16 + e * 8;
  bf16x8 km[9]; s16x4 kt[9];
#pragma unroll
  for (int j = 0; j < 9; ++j) { km[j] = *(const LAS bf16x8*)(L + ka + j * 1024); kt[j] = *(const LAS s16x4*)(L + kb + j * 512); }
  if (h) qb = (s16x4){0, 0, 0, 0};
  auto score = [&](int j, f32x16 c) { return mfma32k8(kt[j], qb, mfma32(km[j], qa, c)); };
  const float NEG = -3.0e38f;
  f32x16 z = {};
  float m;
  {
    f32x16 s8 = score(8, z), s0 = score(0, z);
    float ma = NEG, mb = NEG;
#pragma unroll
    for (int j = 1; j < 8; ++j) {
      f32x16 s = score(j, z);
      if (j == 1) {
#pragma unroll
        for (int r = 0; r < 16; r += 2) {
          const int key = (r & 3) + 8 * (r >> 2) + 4 * h;
          ma = fmaxf(fmaxf(ma, key > q ? s0[r] : s8[r]), key + 1 > q ? s0[r + 1] : s8[r + 1]);
        }
      }
#pragma unroll
      for (int r = 0; r < 16; r += 4) { ma = fmaxf(fmaxf(ma, s[r]), s[r + 1]); mb = fmaxf(fmaxf(mb, s[r + 2]), s[r + 3]); }
    }
    m = fmaxf(ma, mb);
    auto rr = __builtin_amdgcn_permlane32_swap(__float_as_uint(m), __float_as_uint(m), false, false);
    m = fmaxf(__uint_as_float(rr[0]), __uint_as_float(rr[1]));
  }
  f32x16 cm;
#pragma unroll
  for (int r = 0; r < 16; ++r) cm[r] = -m;
  const int blk = (l >> 4) & 1, q4 = (l & 15) >> 2, p = l & 3;
  const unsigned va = (blk ? A_VT + (p == 0 ? e * 8 : 512 + (p >> 1) * 8) : A_VM + (p >> 1) * 512 + (p & 1) * 8) + jb * 1024 + (4 * h + q4) * 16;
  auto rdv = [&](int j, bf16x8& v0, bf16x8& v1) {
    const unsigned off = va + (unsigned)j * 1024;
    v0 = cat8(trd(L + off), trd(L + off + 128)); v1 = cat8(trd(L + off + 256), trd(L + off + 384));
  };
  f32x16 o = {};
  f32x16 sc;
  {
    f32x16 s8 = score(8, cm), s0 = score(0, cm);
    bf16x8 a0, a1, b0, b1;
    rdv(8, a0, a1); rdv(0, b0, b1);
    sc = score(1, cm);
    SB();
    u32x4 pu[2], pl[2];
#pragma unroll
    for (int i = 0; i < 8; ++i) {
      const int key = ((2 * i) & 3) + 8 * ((2 * i) >> 2) + 4 * h;
      const bool u0 = key > q, u1 = key + 1 > q;
      const unsigned pk = pk2(__builtin_amdgcn_exp2f(u0 ? s0[2 * i] : s8[2 * i]), __builtin_amdgcn_exp2f(u1 ? s0[2 * i + 1] : s8[2 * i + 1]));
      const unsigned msk = (u0 ? 0xffffu : 0u) | (u1 ? 0xffff0000u : 0u);
      pu[i >> 2][i & 3] = pk & msk; pl[i >> 2][i & 3] = pk & ~msk;
    }
    SB();
    o = mfma32(a0, __builtin_bit_cast(bf16x8, pl[0]), o); o = mfma32(a1, __builtin_bit_cast(bf16x8, pl[1]), o);
    o = mfma32(b0, __builtin_bit_cast(bf16x8, pu[0]), o); o = mfma32(b1, __builtin_bit_cast(bf16x8, pu[1]), o);
  }
#pragma unroll
  for (int j = 1; j < 8; ++j) {
    bf16x8 v0, v1;
    rdv(j, v0, v1);
    f32x16 sn;
    if (j < 7) sn = score(j + 1, cm);
    SB();
    u32x4 p0, p1;
#pragma unroll
    for (int i = 0; i < 4; ++i) p0[i] = pk2(__builtin_amdgcn_exp2f(sc[2 * i]), __builtin_amdgcn_exp2f(sc[2 * i + 1]));
    o = mfma32(v0, __builtin_bit_cast(bf16x8, p0), o);
#pragma unroll
    for (int i = 0; i < 4; ++i) p1[i] = pk2(__builtin_amdgcn_exp2f(sc[8 + 2 * i]), __builtin_amdgcn_exp2f(sc[8 + 2 * i + 1]));
    o = mfma32(v1, __builtin_bit_cast(bf16x8, p1), o);
    SB();
    if (j < 7) sc = sn;
  }
  attn_store(rws, head, qt, o);
}

constexpr int K2_SYNC = A_END, K2_RED = A_END + 256, K2_LDS = K2_RED + 61440;
__global__ __launch_bounds__(768, 3) void k2_fused(const unsigned short* __restrict__ x, char* __restrict__ ws, unsigned short* __restrict__ out) {
  extern __shared__ __attribute__((aligned(16))) char lds[];
  const int b = blockIdx.x, xcd = b & 7, jj = b >> 3;
  const int tid = threadIdx.x, l = tid & 63, w = __builtin_amdgcn_readfirstlane(tid >> 6);
  const int panel = (xcd >> 1) * 8 + (jj & 7), nb = (xcd & 1) * 4 + (jj >> 3);
  const rsrc_t rws = mkrs(ws, (unsigned)WS_USED);
  ldsp L = (ldsp)0;
  volatile LAS unsigned* sync = (volatile LAS unsigned*)(L + K2_SYNC);
  STAMP(1, 0);
  if (tid == 0) { sync[0] = 0; sync[1] = 0; }
  f32x4 acc[4][5];
  if (w >= 4) {
    const int head = xcd * 4 + (jj & 3), rg = jj >> 2, pr = head >> 1, e = head & 1;
    const int aw = w - 4;
    const int kt0 = 8 * rg - 8;
    {
      const unsigned qb = (unsigned)OFF_Q + (unsigned)(pr * 64 + 8 * rg) * 2560u, kb = (unsigned)OFF_K + (unsigned)(pr * 64 + kt0) * 2560u, vb = (unsigned)OFF_V + (unsigned)(pr * 64 + kt0) * 3072u;
      const unsigned lin = l * 16, two = (l >> 5) * 2560 + (l & 31) * 16;
      const int t0 = rg == 0 ? 8 : 0;
      dma16(A_QM + aw * 1024, lin, rws, qb + aw * 2560 + e * 1024);
      if (aw < 4) dma16(A_QT + aw * 1024, two, rws, qb + aw * 5120 + 2048);
      for (int i = t0 + aw; i < 16; i += 8) dma16(A_KM + i * 1024, lin, rws, kb + i * 2560 + e * 1024);
      if (aw >= (t0 >> 1)) dma16(A_KT + aw * 1024, two, rws, kb + aw * 5120 + 2048);
      for (int i = t0 + aw; i < 16; i += 8) dma16(A_VM + i * 1024, lin, rws, vb + i * 3072 + e * 1024);
      for (int i = t0 + aw; i < 16; i += 8) dma16(A_VT + i * 1024, lin, rws, vb + i * 3072 + 2048);
    }
    WAITBAR(0);
    STAMPW(1, 1, 4);
    if (rg > 0) attn_task_full(rws, head, 8 * rg + aw, aw); else attn_task_short(rws, head, aw, aw);
    STAMPW(1, 2, 4);
    asm volatile("s_waitcnt vmcnt(0)" ::: "memory");
    unsigned old = 0;
    if (l == 0) old = __hip_atomic_fetch_add((LAS unsigned*)(L + K2_SYNC), 1u, __ATOMIC_RELAXED, __HIP_MEMORY_SCOPE_WORKGROUP);
    old = __builtin_amdgcn_readfirstlane(old);
    if (old == 7) {
      GAS unsigned* cnt = (GAS unsigned*)(ws + OFF_CNT + 8192);
      if (l == 0) __hip_atomic_fetch_add(cnt + rg * 64, 1u, RLX_AGENT);
      const GAS unsigned* need = cnt + (panel >> 2) * 64;
      for (unsigned spins = 0; spins < (1u << 22); ++spins) {
        if ((unsigned)__builtin_amdgcn_readfirstlane(__hip_atomic_load(need, RLX_AGENT)) >= 32u) break;
        __builtin_amdgcn_s_sleep(1);
      }
      if (l == 0) sync[1] = 1u;
      STAMPW(1, 3, w);
    }
  } else {
    const unsigned pw = (unsigned)OFF_WT + (unsigned)nb * NP * 5120u + l * 16, pa = (unsigned)OFF_ACT + (unsigned)panel * NP * 4096u + l * 16;
#pragma unroll
    for (int mt = 0; mt < 4; ++mt)
#pragma unroll
      for (int nt = 0; nt < 5; ++nt) acc[mt][nt] = (f32x4){};
    constexpr int DEP = 2;
    bf16x8 fw[DEP][5], fa[DEP][4];
    auto loadW = [&](int d, int P) {
#pragma unroll
      for (int i = 0; i < 5; ++i) fw[d][i] = __builtin_bit_cast(bf16x8, __builtin_amdgcn_raw_buffer_load_b128(rws, pw + i * 1024, P * 5120, 0));
    };
    auto loadA = [&](int d, int P) {
#pragma unroll
      for (int i = 0; i < 4; ++i) fa[d][i] = __builtin_bit_cast(bf16x8, __builtin_amdgcn_raw_buffer_load_b128(rws, pa + i * 1024, P * 4096, 0));
    };
    auto loadO = [&](int d, int P) {
#pragma unroll
      for (int i = 0; i < 4; ++i) fa[d][i] = __builtin_bit_cast(bf16x8, __builtin_amdgcn_raw_buffer_load_b128(rws, pa + i * 1024, P * 4096, AUX_SC1));
    };
    auto mm = [&](int d) {
#pragma unroll
      for (int mt = 0; mt < 4; ++mt)
#pragma unroll
        for (int nt = 0; nt < 5; ++nt) acc[mt][nt] = mfma16(fw[d][nt], fa[d][mt], acc[mt][nt]);
    };
#pragma unroll
    for (int d = 0; d < DEP; ++d) { loadW(d, w + 4 * d); loadA(d, w + 4 * d); }
    WAITBAR(63);
    STAMP(1, 4);
    for (int it = 0; it < 10; it += 2) {
#pragma unroll
      for (int d = 0; d < DEP; ++d) { mm(d); SB(); loadW(d, w + 4 * (it + d + DEP)); loadA(d, w + 4 * (it + d + DEP)); SB(); }
    }
    mm(0); SB(); loadW(0, w + 48); loadA(0, w + 48); SB();
    mm(1); SB();
    {
      const int P = 52 + (w >> 1);
      loadW(1, P);
#pragma unroll
      for (int i = 0; i < 4; ++i) fa[1][i] = __builtin_bit_cast(bf16x8, __builtin_amdgcn_raw_buffer_load_b128(rws, ((i >> 1) == (w & 1)) ? pa + i * 1024 : 0x7ffffff0u, P * 4096, 0));
    }
    SB();
    mm(0); SB(); loadW(0, 54 + w); SB();
    mm(1); SB(); loadW(1, 58 + w); SB();
    STAMP(1, 5);
    while (sync[1] == 0u) __builtin_amdgcn_s_sleep(1);
    STAMP(1, 6);
    loadO(0, 54 + w); loadO(1, 58 + w); SB();
    mm(0); SB(); loadW(0, 62 + w); loadO(0, 62 + w); SB();
    mm(1); SB(); loadW(1, 66 + w); loadO(1, 66 + w); SB();
    mm(0); SB(); loadW(0, 70 + w); loadO(0, 70 + w); SB();
    mm(1); mm(0);
    STAMP(1, 7);
#pragma unroll
    for (int mt = 0; mt < 4; ++mt)
      if (mt != w) {
        const int slot = w < mt ? w : w - 1;
#pragma unroll
        for (int nt = 0; nt < 5; ++nt) *(LAS f32x4*)(L + K2_RED + ((mt * 3 + slot) * 5 + nt) * 1024 + l * 16) = acc[mt][nt];
      }
  }
  __syncthreads();
  if (w < 4) {
    f32x4 r[5];
#pragma unroll
    for (int mt = 0; mt < 4; ++mt)
      if (mt == w) {
#pragma unroll
        for (int nt = 0; nt < 5; ++nt) r[nt] = acc[mt][nt];
      }
#pragma unroll
    for (int s = 0; s < 3; ++s)
#pragma unroll
      for (int nt = 0; nt < 5; ++nt) r[nt] += *(const LAS f32x4*)(L + K2_RED + ((w * 3 + s) * 5 + nt) * 1024 + l * 16);
    const int m16 = l & 15, g = l >> 4;
    const int row = panel * 64 + w * 16 + m16;
    const unsigned short* xr = x + (size_t)row * D + nb * 80;
    unsigned short* orow = out + (size_t)row * D + nb * 80;
#pragma unroll
    for (int pp = 0; pp < 2; ++pp) {
      const u32x4 xv = *(const u32x4*)(xr + 32 * pp + 8 * g);
      float v[8];
#pragma unroll
      for (int e = 0; e < 4; ++e) { v[e] = r[2 * pp][e]; v[4 + e] = r[2 * pp + 1][e]; }
#pragma unroll
      for (int e = 0; e < 4; ++e) { v[2 * e] += lo16(xv[e]); v[2 * e + 1] += hi16(xv[e]); }
      *(u32x4*)(orow + 32 * pp + 8 * g) = (u32x4){pk2(v[0], v[1]), pk2(v[2], v[3]), pk2(v[4], v[5]), pk2(v[6], v[7])};
    }
    const u32x2 xv = *(const u32x2*)(xr + 64 + 4 * g);
    *(u32x2*)(orow + 64 + 4 * g) = (u32x2){pk2(r[4][0] + lo16(xv[0]), r[4][1] + hi16(xv[0])), pk2(r[4][2] + lo16(xv[1]), r[4][3] + hi16(xv[1]))};
  }
  STAMP(2, 0);
}

extern "C" void kernel_launch(void* const* d_in, const int* in_sizes, int n_in, void* d_out, int out_size, void* d_ws, size_t ws_size, hipStream_t stream) {
  const unsigned short* x = (const unsigned short*)d_in[0];
  const unsigned short* wqkv = (const unsigned short*)d_in[1];
  const unsigned short* wo = (const unsigned short*)d_in[2];
  const unsigned short* wg = (const unsigned short*)d_in[4];
  const unsigned short* wu = (const unsigned short*)d_in[5];
  const unsigned short* wd = (const unsigned short*)d_in[6];
  char* ws = (char*)d_ws;
  (void)hipFuncSetAttribute((const void*)k1_gemm1, hipFuncAttributeMaxDynamicSharedMemorySize, K1_LDS);
  (void)hipFuncSetAttribute((const void*)k2_fused, hipFuncAttributeMaxDynamicSharedMemorySize, K2_LDS);
  k1_gemm1<<<256, 512, K1_LDS, stream>>>(x, wqkv, wg, wu, wd, wo, ws);
  k2_fused<<<256, 768, K2_LDS, stream>>>(x, ws, (unsigned short*)d_out);
}
```

```cpp
#include <hip/hip_runtime.h>
#include <hip/hip_bf16.h>

typedef short bf16x8 __attribute__((ext_vector_type(8)));
typedef short s16x4 __attribute__((ext_vector_type(4)));
typedef __bf16 bfv8 __attribute__((ext_vector_type(8)));
typedef __bf16 bfv2 __attribute__((ext_vector_type(2)));
typedef float f32x16 __attribute__((ext_vector_type(16)));
typedef float f32x4 __attribute__((ext_vector_type(4)));
typedef float f32x2 __attribute__((ext_vector_type(2)));
typedef unsigned u32x4 __attribute__((ext_vector_type(4)));
typedef unsigned u32x2 __attribute__((ext_vector_type(2)));
typedef unsigned long long u64;
#define LAS __attribute__((address_space(3)))
#define GAS __attribute__((address_space(1)))
#define DI __device__ __forceinline__
typedef LAS char* ldsp;
typedef __amdgpu_buffer_rsrc_t rsrc_t;
#define RLX_AGENT __ATOMIC_RELAXED, __HIP_MEMORY_SCOPE_AGENT
constexpr int AUX_SC1 = 16;

constexpr int S = 2048, D = 640, F = 1728, NH = 32, DH = 20;
constexpr int NP = 74;
constexpr size_t OFF_Q = 0, QK_BYTES = (size_t)16 * 64 * 2560;
constexpr size_t OFF_K = OFF_Q + QK_BYTES;
constexpr size_t OFF_V = OFF_K + QK_BYTES, V_BYTES = (size_t)16 * 64 * 3072;
constexpr size_t OFF_ACT = OFF_V + V_BYTES, ACT_BYTES = (size_t)32 * NP * 4096;
constexpr size_t OFF_WT = OFF_ACT + ACT_BYTES, WT_BYTES = (size_t)8 * NP * 5120;
constexpr size_t OFF_CSG = OFF_WT + WT_BYTES;
constexpr size_t OFF_RSG = OFF_CSG + (size_t)15 * 10 * 384 * 8;
constexpr size_t OFF_CNT = OFF_RSG + 4096 * 8;
constexpr size_t OFF_FLG = OFF_CNT + 8192;
constexpr size_t WS_USED = OFF_FLG + 512 * 64;
constexpr size_t OFF_STAMP = WS_USED;
#define STAMPW(k, p, wv) do {} while (0)
#define STAMP(k, p) STAMPW(k, p, 0)

DI f32x16 mfma32(bf16x8 a, bf16x8 b, f32x16 c) { return __builtin_amdgcn_mfma_f32_32x32x16_bf16(__builtin_bit_cast(bfv8, a), __builtin_bit_cast(bfv8, b), c, 0, 0, 0); }
DI f32x16 mfma32k8(s16x4 a, s16x4 b, f32x16 c) { return __builtin_amdgcn_mfma_f32_32x32x8bf16_1k(a, b, c, 0, 0, 0); }
DI f32x4 mfma16(bf16x8 a, bf16x8 b, f32x4 c) { return __builtin_amdgcn_mfma_f32_16x16x32_bf16(__builtin_bit_cast(bfv8, a), __builtin_bit_cast(bfv8, b), c, 0, 0, 0); }
DI unsigned pk2(float lo, float hi) { bfv2 v = {(__bf16)lo, (__bf16)hi}; return __builtin_bit_cast(unsigned, v); }
DI float lo16(unsigned v) { return __uint_as_float(v << 16); }
DI float hi16(unsigned v) { return __uint_as_float(v & 0xffff0000u); }
DI float dot2(unsigned a, unsigned b, float c) { return __builtin_amdgcn_fdot2_f32_bf16(__builtin_bit_cast(bfv2, a), __builtin_bit_cast(bfv2, b), c, false); }
DI s16x4 trd(ldsp p) { return __builtin_amdgcn_ds_read_tr16_b64_v4i16((LAS s16x4*)p); }
DI bf16x8 cat8(s16x4 a, s16x4 b) { return __builtin_shufflevector(a, b, 0, 1, 2, 3, 4, 5, 6, 7); }
DI void dma16(unsigned ldsaddr, unsigned voff, rsrc_t rs, unsigned soff) {
  asm volatile("s_mov_b32 m0, %0\n\ts_nop 0\n\tbuffer_load_dwordx4 %1, %2, %3 offen lds" ::"s"(ldsaddr), "v"(voff), "s"(rs), "s"(soff) : "memory");
}
DI void code_prefetch(u64 base, int chunk, unsigned lds_scratch) {
  const u64 a = base + (u64)(chunk * 1024 + (threadIdx.x & 63) * 16);
  asm volatile("s_mov_b32 m0, %0\n\ts_nop 0\n\tglobal_load_lds_dwordx4 %1, off" ::"s"(lds_scratch), "v"(a) : "memory");
}
DI void code_prefetch_at(u64 a, unsigned lds_scratch) {
  asm volatile("s_mov_b32 m0, %0\n\ts_nop 0\n\tglobal_load_lds_dwordx4 %1, off" ::"s"(lds_scratch), "v"(a) : "memory");
}
DI u64 addr_mk() { unsigned lo, hi; asm volatile("s_getpc_b64 s[98:99]\n\ts_add_u32 s98, s98, mk_fused@rel32@lo+4\n\ts_addc_u32 s99, s99, mk_fused@rel32@hi+12\n\ts_mov_b32 %0, s98\n\ts_mov_b32 %1, s99" : "=s"(lo), "=s"(hi) :: "s98", "s99", "scc"); return ((u64)hi << 32) | lo; }
#define CODE_KB 31
DI void dma16s(unsigned ldsaddr, unsigned voff, rsrc_t rs, unsigned soff) {
  asm volatile("s_mov_b32 m0, %0\n\ts_nop 0\n\tbuffer_load_dwordx4 %1, %2, %3 offen sc1 lds" ::"s"(ldsaddr), "v"(voff), "s"(rs), "s"(soff) : "memory");
}
#define BARL() asm volatile("s_waitcnt lgkmcnt(0)\n\ts_barrier" ::: "memory")
#define WAITBAR(N) asm volatile("s_waitcnt vmcnt(" #N ") lgkmcnt(0)\n\ts_barrier" ::: "memory")
#define SB() __builtin_amdgcn_sched_barrier(0)
DI rsrc_t mkrs(const void* p, unsigned bytes) { return __builtin_amdgcn_make_buffer_rsrc((void*)p, 0, bytes, 0x00020000); }
DI u64 ld_gran(const char* p) { return __hip_atomic_load((const GAS u64*)p, RLX_AGENT); }
DI void st_gran(char* p, unsigned tag, float v) { __hip_atomic_store((GAS u64*)p, ((u64)tag << 32) | __float_as_uint(v), RLX_AGENT); }
DI unsigned ld_epoch(const char* p) { unsigned v; asm volatile("s_load_dword %0, %1, 0x0\n\ts_waitcnt lgkmcnt(0)" : "=s"(v) : "s"(p) : "memory"); return v; }
DI unsigned zchunk(unsigned ch) { const unsigned j = (ch * 52u) >> 8, c = ch - 5u * j; return 5u * j + ((0x24310u >> (4u * c)) & 7u); }

constexpr int L_XS = 0, L_WS = 49152, L_RS = 147456, L_CS = 148480, L_PF = 150016, L_SY = 151040, L_TOTAL = 151104;
enum { SY_EPI = 0, SY_QKV = 1, SY_STG = 2, SY_ADONE = 3, SY_O = 4, SY_H = 5, SY_WT = 6 };
template <int NT> struct K1Frag { bf16x8 x[2]; bf16x8 w[NT]; };
struct K1Ctx { unsigned wa[3], xa0, xa1, xb0, xb1; };

template <int NT>
DI void g1_compute(f32x16 (&acc)[2][3], const K1Ctx& c) {
  ldsp L = (ldsp)0;
  auto rdW1 = [&](unsigned off, int n) { return cat8(trd(L + c.wa[n] + off), trd(L + c.wa[n] + off + 1536)); };
  auto rdX1 = [&](unsigned a) { return *(const LAS bf16x8*)(L + a); };
  auto phase = [&](const K1Frag<NT>& cur, K1Frag<NT>& nxt, unsigned xo, unsigned wo) {
    acc[0][0] = mfma32(cur.w[0], cur.x[0], acc[0][0]);
    nxt.x[0] = rdX1(xo); nxt.x[1] = rdX1(xo + 4096);
    SB();
    acc[0][1] = mfma32(cur.w[1], cur.x[0], acc[0][1]);
    nxt.w[0] = rdW1(wo, 0);
    SB();
    if constexpr (NT == 3) acc[0][2] = mfma32(cur.w[2], cur.x[0], acc[0][2]); else acc[1][0] = mfma32(cur.w[0], cur.x[1], acc[1][0]);
    nxt.w[1] = rdW1(wo, 1);
    SB();
    if constexpr (NT == 3) acc[1][0] = mfma32(cur.w[0], cur.x[1], acc[1][0]); else acc[1][1] = mfma32(cur.w[1], cur.x[1], acc[1][1]);
    if constexpr (NT == 3) nxt.w[2] = rdW1(wo, 2);
    SB();
    if constexpr (NT == 3) { acc[1][1] = mfma32(cur.w[1], cur.x[1], acc[1][1]); acc[1][2] = mfma32(cur.w[2], cur.x[1], acc[1][2]); }
    SB();
  };
  K1Frag<NT> FA, FB;
  FA.x[0] = rdX1(c.xa0); FA.x[1] = rdX1(c.xa0 + 4096);
#pragma unroll
  for (int n = 0; n < NT; ++n) FA.w[n] = rdW1(0, n);
  unsigned xs = 0, xsn = 16384, wp = 0;
#pragma unroll 1
  for (int i = 0; i < 10; ++i) {
    SB();
    phase(FA, FB, c.xb0 + xs, wp + 6144);
    BARL();
    phase(FB, FA, c.xa1 + xs, wp + 24576);
    phase(FA, FB, c.xb1 + xs, wp + 24576 + 6144);
    BARL();
    phase(FB, FA, c.xa0 + xsn, wp ^ 49152);
    xs = xsn; xsn = xsn == 32768 ? 0 : xsn + 16384; wp ^= 49152;
  }
}

#define WAITV(N) asm volatile("s_waitcnt vmcnt(" #N ")\n\ts_barrier" ::: "memory")

DI int krow(int P, int slot) {
  const int g4 = slot >> 3, j = slot & 7;
  if (P < 54) return 32 * P + 16 * (g4 >> 1) + 4 * (g4 & 1) + (j & 3) + 8 * (j >> 2);
  const int Pp = P - 54;
  int head, i;
  if (Pp < 16) { head = 2 * Pp + (g4 >> 1); i = (j & 3) + 8 * (j >> 2) + 4 * (g4 & 1); } else { head = 2 * (4 * (Pp - 16) + g4) + (j >> 2); i = 16 + (j & 3); }
  const int d = (head & 1) ? (i < 16 ? i + 4 : i - 16) : i;
  return head * 20 + d;
}

DI float dppadd(float v, int ctrl_sel) {
  int x = __builtin_bit_cast(int, v), y;
  if (ctrl_sel == 0) y = __builtin_amdgcn_update_dpp(0, x, 0xB1, 0xf, 0xf, true);
  else if (ctrl_sel == 1) y = __builtin_amdgcn_update_dpp(0, x, 0x4E, 0xf, 0xf, true);
  else y = __builtin_amdgcn_update_dpp(0, x, 0x141, 0xf, 0xf, true);
  return v + __builtin_bit_cast(float, y);
}

DI void publish(char* ws, int flag, unsigned tag) { __hip_atomic_store((GAS unsigned*)(ws + OFF_FLG + (size_t)flag * 64), tag, RLX_AGENT); }
DI void poll_flags(const char* ws, int flag, unsigned tag) {
  const GAS unsigned* p = (const GAS unsigned*)(ws + OFF_FLG + (size_t)(flag < 0 ? 0 : flag) * 64);
  for (unsigned spins = 0; spins < (1u << 18); ++spins) {
    const bool ok = flag < 0 || __hip_atomic_load(p, RLX_AGENT) == tag;
    if (__all(ok)) break;
    __builtin_amdgcn_s_sleep(1);
  }
}
DI void lds_wait(volatile LAS unsigned* p, unsigned v) { while (*p < v) __builtin_amdgcn_s_sleep(1); }
DI unsigned lds_add(LAS unsigned* p) {
  unsigned old = 0;
  if ((threadIdx.x & 63) == 0) old = __hip_atomic_fetch_add(p, 1u, __ATOMIC_RELAXED, __HIP_MEMORY_SCOPE_WORKGROUP);
  return __builtin_amdgcn_readfirstlane(old);
}

DI void retile(int r  , const unsigned short* __restrict__ wd, const unsigned short* __restrict__ wo, rsrc_t rws) {
  const int tid = threadIdx.x, l = tid & 63, w = __builtin_amdgcn_readfirstlane(tid >> 6);
  ldsp L = (ldsp)0 + w * 2 * 5120;
  const int g4 = l >> 4, q4 = (l & 15) >> 2, p = l & 3;
#pragma unroll 1
  for (int rnd = 0; rnd < 2; ++rnd) {
    u32x4 v[2][5];
#pragma unroll
    for (int uu = 0; uu < 2; ++uu) {
      int unit = (r * 12 + w) + 192 * (2 * rnd + uu); if (unit >= NP * 8) unit = 0;
      const int P = unit >> 3, nb = unit & 7;
      const unsigned short* src = P < 54 ? wd : wo;
#pragma unroll
      for (int i = 0; i < 5; ++i) { const int q = 64 * i + l, slot = q / 10, ch = q - 10 * slot; v[uu][i] = *(const u32x4*)(src + (size_t)krow(P, slot) * D + nb * 80 + ch * 8); }
    }
#pragma unroll
    for (int uu = 0; uu < 2; ++uu)
#pragma unroll
      for (int i = 0; i < 5; ++i) *(LAS u32x4*)(L + uu * 5120 + (64 * i + l) * 16) = v[uu][i];
    asm volatile("s_waitcnt lgkmcnt(0)" ::: "memory");
#pragma unroll
    for (int uu = 0; uu < 2; ++uu) {
      const int unit = (r * 12 + w) + 192 * (2 * rnd + uu);
      if (unit < NP * 8) {
        const int P = unit >> 3, nb = unit & 7;
#pragma unroll
        for (int nt = 0; nt < 5; ++nt) {
          const int colg = nt < 4 ? 32 * (nt >> 1) + 8 * p + 4 * (nt & 1) : 64 + 4 * p;
          ldsp a = L + uu * 5120 + (g4 * 8 + q4) * 160 + colg * 2;
          const bf16x8 f = cat8(trd(a), trd(a + 4 * 160));
          __builtin_amdgcn_raw_buffer_store_b128(__builtin_bit_cast(u32x4, f), rws, (unsigned)OFF_WT + (unsigned)(((nb * NP + P) * 5 + nt) * 1024 + l * 16), 0, AUX_SC1);
        }
      }
    }
    asm volatile("s_waitcnt lgkmcnt(0)" ::: "memory");
  }
}

constexpr int A_KM = 0, A_KT = 16384, A_VM = 24576, A_VT = 40960, A_QM = 57344, A_QT = 65536, A_END = 69632;
DI void attn_store(rsrc_t rws, int head, int qt, const f32x16& o) {
  const int l = threadIdx.x & 63, h = l >> 5, q = l & 31;
  auto rr = __builtin_amdgcn_permlane32_swap(__float_as_uint(o[8]), __float_as_uint(o[8]), false, false);
  const float inv = __builtin_amdgcn_rcpf(__uint_as_float(rr[1]));
  u32x4 ov = {pk2(o[0] * inv, o[1] * inv), pk2(o[2] * inv, o[3] * inv), pk2(o[4] * inv, o[5] * inv), pk2(o[6] * inv, o[7] * inv)};
  const int tok = qt * 32 + q, panel = tok >> 6, mt = (tok >> 4) & 3, m16 = tok & 15;
  const unsigned base = (unsigned)OFF_ACT + ((panel * NP + 54) * 4 + mt) * 1024 + m16 * 16;
  __builtin_amdgcn_raw_buffer_store_b128(ov, rws, base + (head >> 1) * 4096 + ((head & 1) * 2 + h) * 256, 0, AUX_SC1);
  if (!h) __builtin_amdgcn_raw_buffer_store_b64((u32x2){pk2(o[8] * inv, o[9] * inv), pk2(o[10] * inv, o[11] * inv)}, rws,
                                                base + (16 + (head >> 3)) * 4096 + ((head >> 1) & 3) * 256 + (head & 1) * 8, 0, AUX_SC1);
}
DI void attn_task_short(rsrc_t rws, int head, int qt, int ql) {
  const int l = threadIdx.x & 63, h = l >> 5, q = l & 31, e = head & 1;
  const int jb = ql;
  ldsp L = (ldsp)0;
  const bf16x8 qa = *(const LAS bf16x8*)(L + A_QM + ql * 1024 + h * 512 + q * 16);
  s16x4 qb = *(const LAS s16x4*)(L + A_QT + ql * 512 + q * 16 + e * 8);
  if (h) qb = (s16x4){0, 0, 0, 0};
  const unsigned ka = A_KM + jb * 1024 + h * 512 + q * 16, kb = A_KT + jb * 512 + q * 16 + e * 8;
  const int j0 = 8 - qt;
  auto score = [&](int j, f32x16 c) {
    const bf16x8 a = *(const LAS bf16x8*)(L + ka + j * 1024);
    const s16x4 at = *(const LAS s16x4*)(L + kb + j * 512);
    c = mfma32(a, qa, c);
    return mfma32k8(at, qb, c);
  };
  const float NEG = -3.0e38f;
  f32x16 z = {};
  float m = NEG;
  {
    f32x16 s8 = score(8, z);
#pragma unroll
    for (int r = 0; r < 16; ++r) { const int key = (r & 3) + 8 * (r >> 2) + 4 * h; m = fmaxf(m, key > q ? NEG : s8[r]); }
    for (int j = j0; j < 8; ++j) {
      f32x16 s = score(j, z);
#pragma unroll
      for (int r = 0; r < 16; r += 2) m = fmaxf(fmaxf(m, s[r]), s[r + 1]);
    }
    auto rr = __builtin_amdgcn_permlane32_swap(__float_as_uint(m), __float_as_uint(m), false, false);
    m = fmaxf(__uint_as_float(rr[0]), __uint_as_float(rr[1]));
  }
  f32x16 cm;
#pragma unroll
  for (int r = 0; r < 16; ++r) cm[r] = -m;
  const int blk = (l >> 4) & 1, q4 = (l & 15) >> 2, p = l & 3;
  const unsigned va = (blk ? A_VT + (p == 0 ? e * 8 : 512 + (p >> 1) * 8) : A_VM + (p >> 1) * 512 + (p & 1) * 8) + jb * 1024 + (4 * h + q4) * 16;
  f32x16 o = {};
  for (int j = j0; j <= 8; ++j) {
    f32x16 s = score(j, cm);
    u32x4 p0, p1;
#pragma unroll
    for (int i = 0; i < 8; ++i) {
      const int key = ((2 * i) & 3) + 8 * ((2 * i) >> 2) + 4 * h;
      const float e0 = (j == 8 && key > q) ? 0.f : __builtin_amdgcn_exp2f(s[2 * i]), e1 = (j == 8 && key + 1 > q) ? 0.f : __builtin_amdgcn_exp2f(s[2 * i + 1]);
      if (i < 4) p0[i & 3] = pk2(e0, e1); else p1[i & 3] = pk2(e0, e1);
    }
    const unsigned off = va + (unsigned)j * 1024;
    o = mfma32(cat8(trd(L + off), trd(L + off + 128)), __builtin_bit_cast(bf16x8, p0), o);
    o = mfma32(cat8(trd(L + off + 256), trd(L + off + 384)), __builtin_bit_cast(bf16x8, p1), o);
  }
  attn_store(rws, head, qt, o);
}
DI void attn_task_full(rsrc_t rws, int head, int qt, int ql) {
  const int l = threadIdx.x & 63, h = l >> 5, q = l & 31, e = head & 1;
  const int jb = ql;
  ldsp L = (ldsp)0;
  const bf16x8 qa = *(const LAS bf16x8*)(L + A_QM + ql * 1024 + h * 512 + q * 16);
  s16x4 qb = *(const LAS s16x4*)(L + A_QT + ql * 512 + q * 16 + e * 8);
  const unsigned ka = A_KM + jb * 1024 + h * 512 + q * 16, kb = A_KT + jb * 512 + q * 16 + e * 8;
  bf16x8 km[9]; s16x4 kt[9];
#pragma unroll
  for (int j = 0; j < 9; ++j) { km[j] = *(const LAS bf16x8*)(L + ka + j * 1024); kt[j] = *(const LAS s16x4*)(L + kb + j * 512); }
  if (h) qb = (s16x4){0, 0, 0, 0};
  auto score = [&](int j, f32x16 c) { return mfma32k8(kt[j], qb, mfma32(km[j], qa, c)); };
  const float NEG = -3.0e38f;
  f32x16 z = {};
  float m;
  {
    f32x16 s8 = score(8, z), s0 = score(0, z);
    float ma = NEG, mb = NEG;
#pragma unroll
    for (int j = 1; j < 8; ++j) {
      f32x16 s = score(j, z);
      if (j == 1) {
#pragma unroll
        for (int r = 0; r < 16; r += 2) {
          const int key = (r & 3) + 8 * (r >> 2) + 4 * h;
          ma = fmaxf(fmaxf(ma, key > q ? s0[r] : s8[r]), key + 1 > q ? s0[r + 1] : s8[r + 1]);
        }
      }
#pragma unroll
      for (int r = 0; r < 16; r += 4) { ma = fmaxf(fmaxf(ma, s[r]), s[r + 1]); mb = fmaxf(fmaxf(mb, s[r + 2]), s[r + 3]); }
    }
    m = fmaxf(ma, mb);
    auto rr = __builtin_amdgcn_permlane32_swap(__float_as_uint(m), __float_as_uint(m), false, false);
    m = fmaxf(__uint_as_float(rr[0]), __uint_as_float(rr[1]));
  }
  f32x16 cm;
#pragma unroll
  for (int r = 0; r < 16; ++r) cm[r] = -m;
  const int blk = (l >> 4) & 1, q4 = (l & 15) >> 2, p = l & 3;
  const unsigned va = (blk ? A_VT + (p == 0 ? e * 8 : 512 + (p >> 1) * 8) : A_VM + (p >> 1) * 512 + (p & 1) * 8) + jb * 1024 + (4 * h + q4) * 16;
  auto rdv = [&](int j, bf16x8& v0, bf16x8& v1) {
    const unsigned off = va + (unsigned)j * 1024;
    v0 = cat8(trd(L + off), trd(L + off + 128)); v1 = cat8(trd(L + off + 256), trd(L + off + 384));
  };
  f32x16 o = {};
  f32x16 sc;
  {
    f32x16 s8 = score(8, cm), s0 = score(0, cm);
    bf16x8 a0, a1, b0, b1;
    rdv(8, a0, a1); rdv(0, b0, b1);
    sc = score(1, cm);
    SB();
    u32x4 pu[2], pl[2];
#pragma unroll
    for (int i = 0; i < 8; ++i) {
      const int key = ((2 * i) & 3) + 8 * ((2 * i) >> 2) + 4 * h;
      const bool u0 = key > q, u1 = key + 1 > q;
      const unsigned pk = pk2(__builtin_amdgcn_exp2f(u0 ? s0[2 * i] : s8[2 * i]), __builtin_amdgcn_exp2f(u1 ? s0[2 * i + 1] : s8[2 * i + 1]));
      const unsigned msk = (u0 ? 0xffffu : 0u) | (u1 ? 0xffff0000u : 0u);
      pu[i >> 2][i & 3] = pk & msk; pl[i >> 2][i & 3] = pk & ~msk;
    }
    SB();
    o = mfma32(a0, __builtin_bit_cast(bf16x8, pl[0]), o); o = mfma32(a1, __builtin_bit_cast(bf16x8, pl[1]), o);
    o = mfma32(b0, __builtin_bit_cast(bf16x8, pu[0]), o); o = mfma32(b1, __builtin_bit_cast(bf16x8, pu[1]), o);
  }
#pragma unroll
  for (int j = 1; j < 8; ++j) {
    bf16x8 v0, v1;
    rdv(j, v0, v1);
    f32x16 sn;
    if (j < 7) sn = score(j + 1, cm);
    SB();
    u32x4 p0, p1;
#pragma unroll
    for (int i = 0; i < 4; ++i) p0[i] = pk2(__builtin_amdgcn_exp2f(sc[2 * i]), __builtin_amdgcn_exp2f(sc[2 * i + 1]));
    o = mfma32(v0, __builtin_bit_cast(bf16x8, p0), o);
#pragma unroll
    for (int i = 0; i < 4; ++i) p1[i] = pk2(__builtin_amdgcn_exp2f(sc[8 + 2 * i]), __builtin_amdgcn_exp2f(sc[8 + 2 * i + 1]));
    o = mfma32(v1, __builtin_bit_cast(bf16x8, p1), o);
    SB();
    if (j < 7) sc = sn;
  }
  attn_store(rws, head, qt, o);
}

constexpr int L_RED = A_END;
extern "C" __global__ __launch_bounds__(768, 3) void mk_fused(const unsigned short* __restrict__ x, const unsigned short* __restrict__ wqkv,
                                                              const unsigned short* __restrict__ wg, const unsigned short* __restrict__ wu,
                                                              const unsigned short* __restrict__ wd, const unsigned short* __restrict__ wo,
                                                              char* __restrict__ ws, unsigned short* __restrict__ out) {
  extern __shared__ __attribute__((aligned(16))) char lds[];
  const int b = blockIdx.x, xcd = b & 7, jj = b >> 3;
  const int tid = threadIdx.x, l = tid & 63, w = __builtin_amdgcn_readfirstlane(tid >> 6);
  asm volatile("" ::"s"(x), "s"(wqkv), "s"(wg), "s"(wu), "s"(wd), "s"(wo), "s"(ws), "s"(out));
  unsigned epoch;
  ldsp L = (ldsp)0;
  volatile LAS unsigned* sy = (volatile LAS unsigned*)(L + L_SY);
  const rsrc_t rws = mkrs(ws, (unsigned)WS_USED);
  STAMP(0, 0);
  { const u64 ca = addr_mk() + (u64)(w * 1024 + l * 16); code_prefetch_at(ca, L_PF); code_prefetch_at(ca + 12288, L_PF); if (w + 24 < CODE_KB) code_prefetch_at(ca + 24576, L_PF); }
  const bool gemm = jj < 30;
  const int idx = (xcd >> 1) * 30 + jj, cb = idx >> 3, panel = (xcd & 1) * 8 + (idx & 7);
  const bool isffn = cb >= 6;
  const unsigned ldw = isffn ? 3456u : 3840u;
  const int qbase = (cb >> 1) * 640 + (cb & 1) * 320;
  unsigned tag;
  if (gemm) {
    const char* pA = isffn ? (const char*)wg + (cb - 6) * 384 : (const char*)wqkv + qbase * 2;
    const char* pB = isffn ? (const char*)wu + (cb - 6) * 384 : pA;
    const unsigned wbytes = 640u * ldw - (isffn ? (cb - 6) * 384 : qbase * 2);
    const rsrc_t rA = mkrs(pA, wbytes), rB = mkrs(pB, wbytes), rx = mkrs((const char*)x + (size_t)panel * 128 * 1280, 128 * 1280);
    const unsigned qm = isffn ? 0u : ~0u;
    const unsigned ldw32 = 32 * ldw;
    const int c0 = panel < 10 ? panel : panel - 10;
    auto wvoff = [&](unsigned i, unsigned mat) {
      const unsigned q = 64 * i + l, k = (q * 2731u) >> 16, p = q - 24u * k, s = p ^ (2u * (((k >> 1) & 1u) | (mat << 1)));
      const unsigned t = s >> 1, z = zchunk(min(4u * t + 2u * mat + (s & 1u), 39u));
      return k * ldw + ((s + (qm & (z - s))) << 4);
    };
    auto wso = [&](int n) { int st = 2 * c0 + n; st = st >= 20 ? st - 20 : st; return (unsigned)st * ldw32; };
    auto xso = [&](int n) { int ch = c0 + n; ch = ch >= 10 ? ch - 10 : ch; return (unsigned)ch * 128u; };
    {
      const unsigned xr0 = 8 * w + (l >> 3), sx = xso(0), sw0 = wso(0);
      dma16(L_XS + w * 1024, xr0 * 1280 + (((l & 7) ^ ((xr0 >> 1) & 7)) << 4), rx, sx);
      if (w < 4) { const unsigned xr1 = xr0 + 96; dma16(L_XS + (w + 12) * 1024, xr1 * 1280 + (((l & 7) ^ ((xr1 >> 1) & 7)) << 4), rx, sx); }
      else dma16(L_WS + (w - 4) * 1024, wvoff(w - 4, 0), rA, sw0);
      if (w < 4) dma16(L_WS + (w + 8) * 1024, wvoff(w + 8, 0), rA, sw0);
      else dma16(L_WS + 12288 + (w - 4) * 1024, wvoff(w - 4, 1), rB, sw0);
      if (w < 4) dma16(L_WS + 12288 + (w + 8) * 1024, wvoff(w + 8, 1), rB, sw0);
      SB();
    }
    if (__builtin_expect(w >= 8, 1)) {
      const int sw = w - 8;
      const unsigned xrow = 8 * sw + (l >> 3);
      const unsigned vx = xrow * 1280 + (((l & 7) ^ ((xrow >> 1) & 7)) << 4);
      const unsigned dX = L_XS + sw * 1024;
      auto ldx = [&](unsigned slot, unsigned so) {
#pragma unroll
        for (int m = 0; m < 4; ++m) dma16(slot + dX + m * 4096, vx, rx, so + m * 32 * 1280);
      };
      unsigned vw[2][3];
#pragma unroll
      for (int m = 0; m < 3; ++m)
#pragma unroll
        for (int mat = 0; mat < 2; ++mat) vw[mat][m] = wvoff(sw + 4 * m, mat);
      const unsigned dW = L_WS + sw * 1024;
      auto ldw_ = [&](unsigned slot, unsigned so) {
#pragma unroll
        for (int m = 0; m < 3; ++m) { dma16(slot + dW + m * 4096, vw[0][m], rA, so); dma16(slot + dW + 12288 + m * 4096, vw[1][m], rB, so); }
      };
      ldw_(24576, wso(1));
      SB();
      STAMPW(2, 4, 8);
      if (tid == 512) { sy[0] = 0; sy[1] = 0; sy[2] = 0; sy[3] = 0; sy[4] = 0; sy[5] = 0; sy[6] = 0; sy[7] = 0; }
      epoch = ld_epoch(ws + OFF_CNT); tag = epoch + 1u;
      STAMPW(2, 5, 8);
      WAITV(6);
      ldx(16384, xso(1)); ldw_(49152, wso(2)); ldw_(73728, wso(3)); ldx(32768, xso(2));
      SB();
      if (tid == 512) __hip_atomic_store((GAS unsigned*)(ws + OFF_CNT + 4096) + b, tag, RLX_AGENT);
      const bool do_cs = panel < 10 && sw < 3;
      float s8[8];
#pragma unroll
      for (int e = 0; e < 8; ++e) s8[e] = 0.f;
      unsigned sel_lo = 0x00003f80u, sel_hi = 0x3f800000u;
      asm volatile("" : "+s"(sel_lo), "+s"(sel_hi));
      const unsigned csl = 16 * sw + (l >> 2), cmat = csl >= 24 ? 1u : 0u, cs = csl - 24u * cmat, cg = l & 3;
      const unsigned ca0 = L_WS + cmat * 12288 + cg * 384 + ((cs ^ (2u * (((cg >> 1) & 1u) | (cmat << 1)))) << 4);
      auto colsum = [&](unsigned slot) {
        if (do_cs) {
          u32x4 v[8];
#pragma unroll
          for (int i = 0; i < 8; ++i) v[i] = *(const LAS u32x4*)(L + ca0 + slot + i * 1536);
#pragma unroll
          for (int i = 0; i < 8; ++i)
#pragma unroll
            for (int e = 0; e < 4; ++e) { s8[2 * e] = dot2(v[i][e], sel_lo, s8[2 * e]); s8[2 * e + 1] = dot2(v[i][e], sel_hi, s8[2 * e + 1]); }
        }
      };
      colsum(0);
      asm volatile("s_waitcnt lgkmcnt(0)" ::: "memory");
      WAITV(20);
      ldw_(0, wso(4));
      colsum(24576);
      asm volatile("s_waitcnt lgkmcnt(0)" ::: "memory");
      WAITV(16);
      if (do_cs) {
#pragma unroll
        for (int st = 0; st < 2; ++st)
#pragma unroll
          for (int e = 0; e < 8; ++e) s8[e] = dppadd(s8[e], st);
        const float v0 = cg == 0 ? s8[0] : (cg == 1 ? s8[2] : (cg == 2 ? s8[4] : s8[6])), v1 = cg == 0 ? s8[1] : (cg == 1 ? s8[3] : (cg == 2 ? s8[5] : s8[7]));
        char* gp = ws + OFF_CSG + ((size_t)(cb * 10 + c0) * 384 + csl * 8 + 2 * cg) * 8;
        st_gran(gp, tag, v0); st_gran(gp + 8, tag, v1);
      }
      ldw_(24576, wso(5)); ldx(0, xso(3));
      unsigned xs = 16384, wp = 49152;
#pragma unroll 1
      for (int i = 1; i < 8; ++i) {
        WAITV(20);
        ldw_(wp, wso(2 * i + 4));
        WAITV(16);
        ldw_(wp + 24576, wso(2 * i + 5));
        ldx(xs, xso(i < 7 ? i + 3 : 9));
        xs = xs == 32768 ? 0 : xs + 16384; wp ^= 49152;
      }
      WAITV(20);
      const int t = tid - 512;
      u64 g0[2][10], g1;
      unsigned vcs[2];
#pragma unroll
      for (int hh = 0; hh < 2; ++hh) {
        const unsigned dr = min(t + 256 * hh, 383), cu = dr >> 5, ci = dr & 31, pp = (ci >> 2) & 3, hf = ci >> 4;
        const unsigned slot = 2 * cu + (isffn ? (pp >> 1) : (pp & 1)), half = isffn ? (pp & 1) : (pp >> 1);
        vcs[hh] = (unsigned)OFF_CSG + (unsigned)(cb * 10 * 384 + (hf * 24 + slot) * 8 + half * 4 + (ci & 3)) * 8u;
      }
      const unsigned vrs = (unsigned)OFF_RSG + (unsigned)(panel * 256 + t) * 8u;
      auto gather = [&]() {
#pragma unroll
        for (int hh = 0; hh < 2; ++hh)
#pragma unroll
          for (int i = 0; i < 10; ++i) g0[hh][i] = __builtin_bit_cast(u64, __builtin_amdgcn_raw_buffer_load_b64(rws, vcs[hh] + i * 3072, 0, AUX_SC1));
        g1 = __builtin_bit_cast(u64, __builtin_amdgcn_raw_buffer_load_b64(rws, vrs, 0, AUX_SC1));
      };
      STAMPW(2, 1, 8);
      gather();
      WAITV(31);
      WAITV(25);
      asm volatile("s_barrier" ::: "memory");
      STAMPW(2, 2, 8);
      const bool cs_live = isffn || ((t & 255) >> 5) < 10;
      for (unsigned spins = 0; spins < (1u << 18); ++spins) {
        bool ok = (unsigned)(g1 >> 32) == tag;
#pragma unroll
        for (int i = 0; i < 10; ++i) ok &= (unsigned)(g0[0][i] >> 32) == tag;
#pragma unroll
        for (int i = 0; i < 10; ++i) ok &= !isffn && t >= 64 ? true : (unsigned)(g0[1][i] >> 32) == tag;
        if (__all(ok)) break;
        __builtin_amdgcn_s_sleep(2);
        gather();
      }
      (void)cs_live;
      STAMPW(2, 6, 8);
#pragma unroll
      for (int hh = 0; hh < 2; ++hh) {
        float a = 0;
#pragma unroll
        for (int i = 0; i < 10; ++i) a += __uint_as_float((unsigned)g0[hh][i]);
        if (t + 256 * hh < 384) *(LAS float*)(L + L_CS + (t + 256 * hh) * 4) = a;
      }
      *(LAS unsigned*)(L + L_RS + t * 4) = (unsigned)g1;
      BARL();
    } else {
      epoch = ld_epoch(ws + OFF_CNT); tag = epoch + 1u;
      const int wm = w & 1, wn = w >> 1;
      if (!isffn && wn >= 2) {
        const int r6 = l >> 3, srow = min(22 * cb + 6 * (w - 4) + min(r6, 5), 127);
        const char* xp = (const char*)x + (size_t)(panel * 128 + srow) * 1280 + (l & 7) * 16;
        u32x4 sv[10];
#pragma unroll
        for (int j = 0; j < 10; ++j) sv[j] = *(const u32x4*)(xp + j * 128);
        float a0 = 0, a1 = 0, q0 = 0, q1 = 0;
#pragma unroll
        for (int j = 0; j < 10; ++j) { a0 = dot2(sv[j][0], 0x3f803f80u, a0); a1 = dot2(sv[j][1], 0x3f803f80u, a1); a0 = dot2(sv[j][2], 0x3f803f80u, a0); a1 = dot2(sv[j][3], 0x3f803f80u, a1); }
        const float mean = dppadd(dppadd(dppadd(a0 + a1, 0), 1), 2) * (1.f / 640.f);
#pragma unroll
        for (int j = 0; j < 10; ++j)
#pragma unroll
          for (int e = 0; e < 4; ++e) { const float d0 = lo16(sv[j][e]) - mean, d1 = hi16(sv[j][e]) - mean; q0 = fmaf(d0, d0, q0); q1 = fmaf(d1, d1, q1); }
        const float rstd = __builtin_amdgcn_rsqf(dppadd(dppadd(dppadd(q0 + q1, 0), 1), 2) * (1.f / 640.f) + 1e-5f);
        if (r6 < 6 && (l & 7) < 2) st_gran(ws + OFF_RSG + (size_t)((panel * 128 + srow) * 2 + (l & 1)) * 8, tag, (l & 1) ? rstd : mean);
        asm volatile("s_waitcnt vmcnt(1)" ::: "memory");
      } else {
        asm volatile("s_waitcnt vmcnt(0)" ::: "memory");
      }
      const int t0 = isffn ? 3 * wn : (wn < 2 ? 3 * wn : 2 * wn + 2);
      const bool three = isffn || wn < 2;
      const int h = l >> 5, blk = (l >> 4) & 1, q4 = (l & 15) >> 2, p = l & 3;
      K1Ctx c;
      {
        const unsigned swz = 2 * (((q4 >> 1) & 1) | (blk << 1));
        const int pc = isffn ? (p >> 1) : (p & 1), ph = isffn ? (p & 1) : (p >> 1);
#pragma unroll
        for (int nt = 0; nt < 3; ++nt) c.wa[nt] = (unsigned)(L_WS + blk * 12288 + (8 * h + q4) * 384 + (((2 * (t0 + nt) + pc) ^ swz) << 4) + ph * 8);
        const int xr = 64 * wm + (l & 31);
        auto xadr = [&](int ks4) { return (unsigned)(L_XS + xr * 128 + (((2 * ks4 + h) ^ ((xr >> 1) & 7)) << 4)); };
        c.xa0 = xadr(0); c.xb0 = xadr(1); c.xa1 = xadr(2); c.xb1 = xadr(3);
      }
      f32x16 acc[2][3];
#pragma unroll
      for (int mt = 0; mt < 2; ++mt)
#pragma unroll
        for (int nt = 0; nt < 3; ++nt) acc[mt][nt] = (f32x16){};
      STAMP(2, 3);
      BARL();
      STAMP(0, 1);
      if (three) g1_compute<3>(acc, c); else g1_compute<2>(acc, c);
      STAMP(0, 2);
      BARL();
      STAMP(2, 7);
      const int m32 = l & 31;
      f32x2 st[2];
#pragma unroll
      for (int mt = 0; mt < 2; ++mt) st[mt] = *(const LAS f32x2*)(L + L_RS + (64 * wm + 32 * mt + m32) * 8);
#pragma unroll
      for (int nt = 0; nt < 3; ++nt) {
        if (nt < 2 || three) {
          const int u = t0 + nt;
          float cc[16];
#pragma unroll
          for (int g = 0; g < 4; ++g) { f32x4 t = *(const LAS f32x4*)(L + L_CS + (u * 32 + 8 * g + 4 * h) * 4); cc[4 * g] = t[0]; cc[4 * g + 1] = t[1]; cc[4 * g + 2] = t[2]; cc[4 * g + 3] = t[3]; }
          if (isffn) {
            const int U = (cb - 6) * 12 + u, P = U >> 1, g4 = (U & 1) * 2 + h;
            const unsigned off = (unsigned)OFF_ACT + (((panel * 2 + wm) * NP + P) * 4 + (m32 >> 4)) * 1024 + g4 * 256 + (m32 & 15) * 16;
#pragma unroll
            for (int mt = 0; mt < 2; ++mt) {
              const float nm = -st[mt][0], r2 = st[mt][1] * st[mt][1];
              float hv[8];
#pragma unroll
              for (int j = 0; j < 8; ++j) hv[j] = (fmaf(nm, cc[j], acc[mt][nt][j]) * r2) * fmaf(nm, cc[j + 8], acc[mt][nt][j + 8]);
              u32x4 o = {pk2(hv[0], hv[1]), pk2(hv[2], hv[3]), pk2(hv[4], hv[5]), pk2(hv[6], hv[7])};
              __builtin_amdgcn_raw_buffer_store_b128(o, rws, off + mt * 2048, 0, AUX_SC1);
            }
          } else {
            const int mat = cb >> 1;
            const unsigned TS = mat == 2 ? 3072u : 2560u;
            const unsigned mbase = (unsigned)(mat == 2 ? OFF_V : (mat == 1 ? OFF_K : OFF_Q)) + (unsigned)((cb & 1) * 8 * 64 + panel * 4 + 2 * wm) * TS + m32 * 16;
            const float qs = mat == 0 ? 0.22360679774997896f * 1.4426950408889634f : 1.f;
#pragma unroll
            for (int hf = 0; hf < 2; ++hf) {
              const int ch = 4 * u + 2 * hf + h, j = (ch * 52) >> 8, cix = ch - 5 * j;
              const unsigned off = mbase + (unsigned)j * 64u * TS + cix * 512;
#pragma unroll
              for (int mt = 0; mt < 2; ++mt) {
                const float nm = -st[mt][0], sc = st[mt][1] * qs;
                float v[8];
#pragma unroll
                for (int e = 0; e < 8; ++e) v[e] = fmaf(nm, cc[8 * hf + e], acc[mt][nt][8 * hf + e]) * sc;
                u32x4 o = {pk2(v[0], v[1]), pk2(v[2], v[3]), pk2(v[4], v[5]), pk2(v[6], v[7])};
                __builtin_amdgcn_raw_buffer_store_b128(o, rws, off + mt * TS, 0, AUX_SC1);
                if (mat == 2 && cix == 4) __builtin_amdgcn_raw_buffer_store_b128((u32x4){0x00003f80u, 0u, 0u, 0u}, rws, off + mt * TS + 512, 0, AUX_SC1);
              }
            }
          }
        }
      }
      STAMP(0, 3);
      asm volatile("s_waitcnt vmcnt(0)" ::: "memory");
      if (lds_add((LAS unsigned*)(L + L_SY) + SY_EPI) == 7u && l == 0) publish(ws, cb * 16 + panel, tag);
      STAMP(0, 4);
    }
  } else {
    const int r = xcd * 2 + (jj - 30);
    if (tid == 0) { sy[0] = 0; sy[1] = 0; sy[2] = 0; sy[3] = 0; sy[4] = 0; sy[5] = 0; sy[6] = 0; sy[7] = 0; }
    epoch = ld_epoch(ws + OFF_CNT); tag = epoch + 1u;
    BARL();
    if (tid == 0) __hip_atomic_store((GAS unsigned*)(ws + OFF_CNT + 4096) + b, tag, RLX_AGENT);
    retile(r, wd, wo, rws);
    asm volatile("s_waitcnt vmcnt(0)" ::: "memory");
    if (lds_add((LAS unsigned*)(L + L_SY) + SY_EPI) == 11u && l == 0) publish(ws, 240 + r, tag);
    if (r == 0 && w == 11) {
      const GAS unsigned* st = (const GAS unsigned*)(ws + OFF_CNT + 4096);
      for (unsigned spins = 0; spins < (1u << 18); ++spins) {
        bool ok = true;
#pragma unroll
        for (int i = 0; i < 4; ++i) ok &= __hip_atomic_load(st + i * 64 + l, RLX_AGENT) == tag;
        if (__all(ok)) break;
        __builtin_amdgcn_s_sleep(2);
      }
      if (l == 0) __hip_atomic_store((GAS unsigned*)(ws + OFF_CNT), tag, RLX_AGENT);
    }
    BARL();
    STAMP(0, 5);
  }

  const int panel2 = (xcd >> 1) * 8 + (jj & 7), nb = (xcd & 1) * 4 + (jj >> 3);
  f32x4 acc2[4][5];
  if (w >= 4) {
    const int head = xcd * 4 + (jj & 3), rg = jj >> 2, pr = head >> 1, e = head & 1;
    const int aw = w - 4;
    const int kt0 = 8 * rg - 8;
    if (w == 8) {
      int f = -1;
      if (l < 2) f = (pr >> 3) * 16 + 2 * rg + l;
      else if (l < 10) { const int pn = 2 * rg - 2 + ((l - 2) & 3); f = pn >= 0 ? ((l < 6 ? 2 : 4) + (pr >> 3)) * 16 + pn : -1; }
      poll_flags(ws, f, tag);
      if (l == 0) sy[SY_QKV] = 1u;
    }
    lds_wait(sy + SY_QKV, 1u);
    STAMPW(1, 0, 8);
    {
      const unsigned qb = (unsigned)OFF_Q + (unsigned)(pr * 64 + 8 * rg) * 2560u, kb = (unsigned)OFF_K + (unsigned)(pr * 64 + kt0) * 2560u, vb = (unsigned)OFF_V + (unsigned)(pr * 64 + kt0) * 3072u;
      const unsigned lin = l * 16, two = (l >> 5) * 2560 + (l & 31) * 16;
      const int t0 = rg == 0 ? 8 : 0;
      dma16s(A_QM + aw * 1024, lin, rws, qb + aw * 2560 + e * 1024);
      if (aw < 4) dma16s(A_QT + aw * 1024, two, rws, qb + aw * 5120 + 2048);
      for (int i = t0 + aw; i < 16; i += 8) dma16s(A_KM + i * 1024, lin, rws, kb + i * 2560 + e * 1024);
      if (aw >= (t0 >> 1)) dma16s(A_KT + aw * 1024, two, rws, kb + aw * 5120 + 2048);
      for (int i = t0 + aw; i < 16; i += 8) dma16s(A_VM + i * 1024, lin, rws, vb + i * 3072 + e * 1024);
      for (int i = t0 + aw; i < 16; i += 8) dma16s(A_VT + i * 1024, lin, rws, vb + i * 3072 + 2048);
    }
    asm volatile("s_waitcnt vmcnt(0)" ::: "memory");
    lds_add((LAS unsigned*)(L + L_SY) + SY_STG);
    lds_wait(sy + SY_STG, 8u);
    asm volatile("" ::: "memory");
    STAMPW(1, 1, 8);
    if (rg > 0) attn_task_full(rws, head, 8 * rg + aw, aw); else attn_task_short(rws, head, aw, aw);
    STAMPW(1, 2, 8);
    asm volatile("s_waitcnt vmcnt(0)" ::: "memory");
    if (lds_add((LAS unsigned*)(L + L_SY) + SY_ADONE) == 7u) {
      if (l == 0) publish(ws, 256 + head * 8 + rg, tag);
      poll_flags(ws, l < 32 ? 256 + l * 8 + (panel2 >> 2) : -1, tag);
      if (l == 0) sy[SY_O] = 1u;
      STAMPW(1, 3, w);
    }
  } else {
    const unsigned pw = (unsigned)OFF_WT + (unsigned)nb * NP * 5120u + l * 16, pa = (unsigned)OFF_ACT + (unsigned)panel2 * NP * 4096u + l * 16;
#pragma unroll
    for (int mt = 0; mt < 4; ++mt)
#pragma unroll
      for (int nt = 0; nt < 5; ++nt) acc2[mt][nt] = (f32x4){};
    constexpr int DEP = 2;
    bf16x8 fw[DEP][5], fa[DEP][4];
    auto loadW = [&](int d, int P) {
#pragma unroll
      for (int i = 0; i < 5; ++i) fw[d][i] = __builtin_bit_cast(bf16x8, __builtin_amdgcn_raw_buffer_load_b128(rws, pw + i * 1024, P * 5120, AUX_SC1));
    };
    auto loadA = [&](int d, int P) {
#pragma unroll
      for (int i = 0; i < 4; ++i) fa[d][i] = __builtin_bit_cast(bf16x8, __builtin_amdgcn_raw_buffer_load_b128(rws, pa + i * 1024, P * 4096, AUX_SC1));
    };
    auto mm = [&](int d) {
#pragma unroll
      for (int mt = 0; mt < 4; ++mt)
#pragma unroll
        for (int nt = 0; nt < 5; ++nt) acc2[mt][nt] = mfma16(fw[d][nt], fa[d][mt], acc2[mt][nt]);
    };
    const int rot = ((panel2 & 7) * 5 + (nb & 3) * 3) % 13;
    auto Ph = [&](int i) { int r_ = i + rot; r_ = r_ >= 13 ? r_ - 13 : r_; return w + 4 * r_; };
    if (w == 0) {
      poll_flags(ws, l < 16 ? 240 + l : -1, tag);
      if (l == 0) sy[SY_WT] = 1u;
    } else lds_wait(sy + SY_WT, 1u);
    asm volatile("" ::: "memory");
    loadW(0, Ph(0)); loadW(1, Ph(1));
    if (w == 0) {
      poll_flags(ws, l < 9 ? (6 + l) * 16 + (panel2 >> 1) : -1, tag);
      if (l == 0) sy[SY_H] = 1u;
    } else lds_wait(sy + SY_H, 1u);
    asm volatile("" ::: "memory");
    STAMP(1, 4);
    loadA(0, Ph(0)); loadA(1, Ph(1));
#pragma unroll 1
    for (int it = 0; it < 10; it += 2) {
#pragma unroll
      for (int d = 0; d < DEP; ++d) { mm(d); SB(); loadW(d, Ph(it + d + DEP)); loadA(d, Ph(it + d + DEP)); SB(); }
    }
    mm(0); SB(); loadW(0, Ph(12)); loadA(0, Ph(12)); SB();
    mm(1); SB();
    {
      const int P = 52 + (w >> 1);
      loadW(1, P);
#pragma unroll
      for (int i = 0; i < 4; ++i) fa[1][i] = __builtin_bit_cast(bf16x8, __builtin_amdgcn_raw_buffer_load_b128(rws, ((i >> 1) == (w & 1)) ? pa + i * 1024 : 0x7ffffff0u, P * 4096, AUX_SC1));
    }
    SB();
    mm(0); SB(); loadW(0, 54 + w); SB();
    mm(1); SB(); loadW(1, 58 + w); SB();
    STAMP(1, 5);
    lds_wait(sy + SY_O, 1u);
    asm volatile("" ::: "memory");
    STAMP(1, 6);
    loadA(0, 54 + w); loadA(1, 58 + w); SB();
    mm(0); SB(); loadW(0, 62 + w); loadA(0, 62 + w); SB();
    mm(1); SB(); loadW(1, 66 + w); loadA(1, 66 + w); SB();
    mm(0); SB(); loadW(0, 70 + w); loadA(0, 70 + w); SB();
    mm(1); mm(0);
    STAMP(1, 7);
#pragma unroll
    for (int mt = 0; mt < 4; ++mt)
      if (mt != w) {
        const int slot = w < mt ? w : w - 1;
#pragma unroll
        for (int nt = 0; nt < 5; ++nt) *(LAS f32x4*)(L + L_RED + ((mt * 3 + slot) * 5 + nt) * 1024 + l * 16) = acc2[mt][nt];
      }
    asm volatile("s_waitcnt lgkmcnt(0)" ::: "memory");
    lds_add((LAS unsigned*)(L + L_SY) + SY_EPI + 7);
    lds_wait(sy + 7, 4u);
    asm volatile("" ::: "memory");
    f32x4 r[5];
#pragma unroll
    for (int mt = 0; mt < 4; ++mt)
      if (mt == w) {
#pragma unroll
        for (int nt = 0; nt < 5; ++nt) r[nt] = acc2[mt][nt];
      }
#pragma unroll
    for (int s = 0; s < 3; ++s)
#pragma unroll
      for (int nt = 0; nt < 5; ++nt) r[nt] += *(const LAS f32x4*)(L + L_RED + ((w * 3 + s) * 5 + nt) * 1024 + l * 16);
    const int m16 = l & 15, g = l >> 4;
    const int row = panel2 * 64 + w * 16 + m16;
    const unsigned short* xr = x + (size_t)row * D + nb * 80;
    unsigned short* orow = out + (size_t)row * D + nb * 80;
#pragma unroll
    for (int pp = 0; pp < 2; ++pp) {
      const u32x4 xv = *(const u32x4*)(xr + 32 * pp + 8 * g);
      float v[8];
#pragma unroll
      for (int e = 0; e < 4; ++e) { v[e] = r[2 * pp][e]; v[4 + e] = r[2 * pp + 1][e]; }
#pragma unroll
      for (int e = 0; e < 4; ++e) { v[2 * e] += lo16(xv[e]); v[2 * e + 1] += hi16(xv[e]); }
      *(u32x4*)(orow + 32 * pp + 8 * g) = (u32x4){pk2(v[0], v[1]), pk2(v[2], v[3]), pk2(v[4], v[5]), pk2(v[6], v[7])};
    }
    const u32x2 xv = *(const u32x2*)(xr + 64 + 4 * g);
    *(u32x2*)(orow + 64 + 4 * g) = (u32x2){pk2(r[4][0] + lo16(xv[0]), r[4][1] + hi16(xv[0])), pk2(r[4][2] + lo16(xv[1]), r[4][3] + hi16(xv[1]))};
    STAMP(2, 0);
  }
}

extern "C" void kernel_launch(void* const* d_in, const int* in_sizes, int n_in, void* d_out, int out_size, void* d_ws, size_t ws_size, hipStream_t stream) {
  const unsigned short* x = (const unsigned short*)d_in[0];
  const unsigned short* wqkv = (const unsigned short*)d_in[1];
  const unsigned short* wo = (const unsigned short*)d_in[2];
  const unsigned short* wg = (const unsigned short*)d_in[4];
  const unsigned short* wu = (const unsigned short*)d_in[5];
  const unsigned short* wd = (const unsigned short*)d_in[6];
  (void)hipFuncSetAttribute((const void*)mk_fused, hipFuncAttributeMaxDynamicSharedMemorySize, L_TOTAL);
  mk_fused<<<256, 768, L_TOTAL, stream>>>(x, wqkv, wg, wu, wd, wo, (char*)d_ws, (unsigned short*)d_out);
}
```
